```python
import torch

def _softmax(x, axis=-1):
    return torch.softmax(x, dim=axis)

def _gelu(x):
    return torch.nn.functional.gelu(x, approximate='tanh')

def _silu(x):
    return torch.nn.functional.silu(x)

def _sigmoid(x):
    return torch.sigmoid(x)

def _layernorm(x, axis=-1, eps=1e-05):
    m = x.mean(dim=axis, keepdim=True)
    v = x.var(dim=axis, keepdim=True, unbiased=False)
    return (x - m) / torch.sqrt(v + eps)

def _scatter_add(tgt, idx, src, axis=0):
    return tgt.clone().index_add_(axis, idx, src)

def _segment_sum(src, seg, n):
    out = torch.zeros((n,) + src.shape[1:], dtype=src.dtype)
    return out.index_add_(0, seg, src)

def _pad(x, pw):
    flat = tuple((v for pair in reversed(pw) for v in pair))
    return torch.nn.functional.pad(x, flat)
kernel_name = 'standalone__kv_cache_quant_pre_rope_vs_post_rope__prompt'

def setup_inputs():

    _w = lambda a: (lambda t: t.to(torch.bfloat16) if t.is_floating_point() else t)(torch.as_tensor(a))
    B, Sq, Skv = (1, 2048, 2048)
    H, Dh0 = (32, 128)
    G = 32 if 32 > 0 else Dh0
    G = max(2, G // 2 * 2)
    Dh = max(G, Dh0 // G * G)
    Skv = max(Skv, Sq)
    ng = Dh // G
    bits = 4 if 4 > 1 else 4
    bits = max(2, min(8, bits))
    qmax = (1 << bits) - 1
    half = Dh // 2
    inv = 1.0 / 10000.0 ** (torch.arange(half) / max(1, half))
    ang_k = torch.arange(Skv)[:, None] * inv[None, :]
    ang_q = torch.arange(Skv - Sq, Skv)[:, None] * inv[None, :]
    kc = torch.cat([torch.cos(ang_k), torch.cos(ang_k)], dim=-1)
    ks = torch.cat([torch.sin(ang_k), torch.sin(ang_k)], dim=-1)
    qc = torch.cat([torch.cos(ang_q), torch.cos(ang_q)], dim=-1)
    qs = torch.cat([torch.sin(ang_q), torch.sin(ang_q)], dim=-1)
    qk_i = torch.randint(0, qmax + 1, (B, H, Skv, Dh)).to(torch.int32)
    qv_i = torch.randint(0, qmax + 1, (B, H, Skv, Dh)).to(torch.int32)
    ksc = torch.abs((lambda *s: torch.randn(*s, dtype=torch.bfloat16) * 0.2)(B, H, Skv, ng)) * 0.1 + 0.01
    kzp = torch.full((B, H, Skv, ng), qmax / 2.0)
    vsc = torch.abs((lambda *s: torch.randn(*s, dtype=torch.bfloat16) * 0.2)(B, H, Skv, ng)) * 0.1 + 0.01
    vzp = torch.full((B, H, Skv, ng), qmax / 2.0)
    return {'q': _w((lambda *s: torch.randn(*s, dtype=torch.bfloat16) * 6.4)(B, H, Sq, Dh) * 0.5), 'qk': _w(qk_i), 'k_scales': _w(ksc), 'k_zeros': _w(kzp), 'qv': _w(qv_i), 'v_scales': _w(vsc), 'v_zeros': _w(vzp), 'q_cos': _w(qc), 'q_sin': _w(qs), 'k_cos': _w(kc), 'k_sin': _w(ks)}

def reference(q, qk, k_scales, k_zeros, qv, v_scales, v_zeros, q_cos, q_sin, k_cos, k_sin):
    B, H, Sq, Dh = q.shape
    ng = k_scales.shape[-1]
    G = Dh // ng
    half = Dh // 2
    ks = (lambda x, n, axis: torch.repeat_interleave(x, n, dim=axis))(k_scales, G, axis=-1)
    kz = (lambda x, n, axis: torch.repeat_interleave(x, n, dim=axis))(k_zeros, G, axis=-1)
    k = ((lambda x, dt: x.to(dt))(qk, q.dtype) - kz) * ks
    vs = (lambda x, n, axis: torch.repeat_interleave(x, n, dim=axis))(v_scales, G, axis=-1)
    vz = (lambda x, n, axis: torch.repeat_interleave(x, n, dim=axis))(v_zeros, G, axis=-1)
    v = ((lambda x, dt: x.to(dt))(qv, q.dtype) - vz) * vs
    k1 = k[..., :half].clone()
    k2 = k[..., half:].clone()
    krh = torch.concatenate([-k2, k1], axis=-1)
    kr = k * k_cos[None, None, :, :] + krh * k_sin[None, None, :, :]
    q1 = q[..., :half].clone()
    q2 = q[..., half:].clone()
    qrh = torch.concatenate([-q2, q1], axis=-1)
    qr = q * q_cos[None, None, :, :] + qrh * q_sin[None, None, :, :]
    logits = torch.einsum('bhqd,bhkd->bhqk', qr, kr) * float(Dh) ** (-0.5)
    attn = _softmax(logits, axis=-1)
    out = torch.einsum('bhqk,bhkd->bhqd', attn, v)
    return out
```

```cpp
#include <hip/hip_runtime.h>
typedef unsigned short u16;
typedef unsigned u32x2 __attribute__((ext_vector_type(2)));
typedef unsigned u32x4 __attribute__((ext_vector_type(4)));
typedef int i32x4 __attribute__((ext_vector_type(4)));
typedef unsigned short u16x4 __attribute__((ext_vector_type(4)));
typedef float f32x2 __attribute__((ext_vector_type(2)));
typedef float f32x4 __attribute__((ext_vector_type(4)));
typedef __bf16 bf16x2 __attribute__((ext_vector_type(2)));
typedef __bf16 bf16x8 __attribute__((ext_vector_type(8)));
#define DI __device__ __forceinline__
DI unsigned cvtpk(float lo, float hi) { f32x2 v = {lo, hi}; bf16x2 b = __builtin_convertvector(v, bf16x2); return __builtin_bit_cast(unsigned, b); }
DI float bf2f(u16 x) { return __uint_as_float(((unsigned)x) << 16); }

DI bf16x8 rope_chunk(const float (&a)[4], const float (&b)[4], const u16* __restrict__ cs, const u16* __restrict__ sn, unsigned c) {
  const u16x4 c1 = *(const u16x4*)(cs + 4 * c), c2 = *(const u16x4*)(cs + 64 + 4 * c), s1 = *(const u16x4*)(sn + 4 * c), s2 = *(const u16x4*)(sn + 64 + 4 * c);
  float o1[4], o2[4];
  #pragma unroll
  for (int e = 0; e < 4; ++e) { o1[e] = a[e] * bf2f(c1[e]) - b[e] * bf2f(s1[e]); o2[e] = b[e] * bf2f(c2[e]) + a[e] * bf2f(s2[e]); }
  u32x4 w = {cvtpk(o1[0], o1[1]), cvtpk(o1[2], o1[3]), cvtpk(o2[0], o2[1]), cvtpk(o2[2], o2[3])};
  return __builtin_bit_cast(bf16x8, w);
}

DI void slow_rows_body(const u16* __restrict__ q, const int* __restrict__ qk, const u16* __restrict__ ks, const u16* __restrict__ kz,
    const int* __restrict__ qv, const u16* __restrict__ vs, const u16* __restrict__ vz, const u16* __restrict__ qc, const u16* __restrict__ qs,
    const u16* __restrict__ kc, const u16* __restrict__ ksn, u16* __restrict__ out, unsigned h, unsigned row0) {
  const unsigned lane = __builtin_amdgcn_mbcnt_hi(~0u, __builtin_amdgcn_mbcnt_lo(~0u, 0u)), r = lane & 15u, G = lane >> 4;
  const float CS = 0.12751743082f;
  constexpr int NB = 4;
  #pragma unroll 1
  for (unsigned qb0 = 0; qb0 < 4; qb0 += NB) {
    bf16x8 qf[NB][4];
    f32x4 o[NB][8];
    float m[NB], l[NB];
    #pragma unroll
    for (int b = 0; b < NB; ++b) {
      const unsigned qrow = row0 + 16u * (qb0 + b) + r;
      #pragma unroll
      for (int ds = 0; ds < 4; ++ds) {
        const unsigned c = 4u * ds + G;
        const u16* qp = q + ((size_t)h * 2048u + qrow) * 128u;
        const u16x4 x = *(const u16x4*)(qp + 4 * c), y = *(const u16x4*)(qp + 64 + 4 * c);
        float a[4], bb[4];
        #pragma unroll
        for (int e = 0; e < 4; ++e) { a[e] = bf2f(x[e]); bb[e] = bf2f(y[e]); }
        qf[b][ds] = rope_chunk(a, bb, qc + qrow * 128u, qs + qrow * 128u, c);
      }
      #pragma unroll
      for (int db = 0; db < 8; ++db) o[b][db] = f32x4{0.f, 0.f, 0.f, 0.f};
      m[b] = -__builtin_inff(); l[b] = 0.f;
    }
    #pragma unroll 1
    for (unsigned k0 = 0; k0 < 2048u; k0 += 32u) {
      f32x4 s[NB][2];
      #pragma unroll
      for (int kb = 0; kb < 2; ++kb) {
        const unsigned key = k0 + 16u * kb + r; const size_t krow = (size_t)h * 2048u + key;
        const u16x4 ksc = *(const u16x4*)(ks + krow * 4u), kzp = *(const u16x4*)(kz + krow * 4u);
        #pragma unroll
        for (int b = 0; b < NB; ++b) s[b][kb] = f32x4{0.f, 0.f, 0.f, 0.f};
        #pragma unroll
        for (int ds = 0; ds < 4; ++ds) {
          const unsigned c = 4u * ds + G; const int g = ds >> 1;
          const i32x4 x = *(const i32x4*)(qk + krow * 128u + 4 * c), y = *(const i32x4*)(qk + krow * 128u + 64 + 4 * c);
          const float s1 = bf2f(ksc[g]) * CS, s2 = bf2f(ksc[g + 2]) * CS, z1 = bf2f(kzp[g]), z2 = bf2f(kzp[g + 2]);
          float a[4], bb[4];
          #pragma unroll
          for (int e = 0; e < 4; ++e) { a[e] = ((float)x[e] - z1) * s1; bb[e] = ((float)y[e] - z2) * s2; }
          const bf16x8 kf = rope_chunk(a, bb, kc + key * 128u, ksn + key * 128u, c);
          #pragma unroll
          for (int b = 0; b < NB; ++b) s[b][kb] = __builtin_amdgcn_mfma_f32_16x16x32_bf16(kf, qf[b][ds], s[b][kb], 0, 0, 0);
        }
      }
      bf16x8 pf[NB]; float alpha[NB];
      #pragma unroll
      for (int b = 0; b < NB; ++b) {
        float tm = fmaxf(fmaxf(fmaxf(s[b][0][0], s[b][0][1]), fmaxf(s[b][0][2], s[b][0][3])), fmaxf(fmaxf(s[b][1][0], s[b][1][1]), fmaxf(s[b][1][2], s[b][1][3])));
        tm = fmaxf(tm, __shfl_xor(tm, 16)); tm = fmaxf(tm, __shfl_xor(tm, 32));
        const float mn = fmaxf(m[b], tm), mref = (mn == -__builtin_inff()) ? 0.f : mn;
        alpha[b] = __builtin_amdgcn_exp2f(m[b] - mref);
        m[b] = mn;
        float p[8];
        #pragma unroll
        for (int e = 0; e < 8; ++e) p[e] = __builtin_amdgcn_exp2f(s[b][e >> 2][e & 3] - mref);
        u32x4 pw = {cvtpk(p[0], p[1]), cvtpk(p[2], p[3]), cvtpk(p[4], p[5]), cvtpk(p[6], p[7])};
        float ps = 0.f;
        #pragma unroll
        for (int e = 0; e < 4; ++e) ps += __uint_as_float(pw[e] << 16) + __uint_as_float(pw[e] & 0xffff0000u);
        l[b] = l[b] * alpha[b] + ps;
        pf[b] = __builtin_bit_cast(bf16x8, pw);
      }
      u16x4 vsc[8], vzp[8];
      #pragma unroll
      for (int j = 0; j < 8; ++j) {
        const size_t vrow = (size_t)h * 2048u + k0 + 16u * (j >> 2) + 4u * G + (j & 3);
        vsc[j] = *(const u16x4*)(vs + vrow * 4u); vzp[j] = *(const u16x4*)(vz + vrow * 4u);
      }
      #pragma unroll
      for (int db = 0; db < 8; ++db) {
        const unsigned d = 16u * db + r;
        float v[8];
        #pragma unroll
        for (int j = 0; j < 8; ++j) {
          const size_t vrow = (size_t)h * 2048u + k0 + 16u * (j >> 2) + 4u * G + (j & 3);
          v[j] = ((float)qv[vrow * 128u + d] - bf2f(vzp[j][db >> 1])) * bf2f(vsc[j][db >> 1]);
        }
        u32x4 vw = {cvtpk(v[0], v[1]), cvtpk(v[2], v[3]), cvtpk(v[4], v[5]), cvtpk(v[6], v[7])};
        #pragma unroll
        for (int b = 0; b < NB; ++b) {
          o[b][db] *= alpha[b];
          o[b][db] = __builtin_amdgcn_mfma_f32_16x16x32_bf16(__builtin_bit_cast(bf16x8, vw), pf[b], o[b][db], 0, 0, 0);
        }
      }
    }
    #pragma unroll
    for (int b = 0; b < NB; ++b) {
      float lt = l[b]; lt += __shfl_xor(lt, 16); lt += __shfl_xor(lt, 32);
      const float il = 1.0f / lt;
      u16* op = out + ((size_t)h * 2048u + row0 + 16u * (qb0 + b) + r) * 128u + 4u * G;
      #pragma unroll
      for (int db = 0; db < 8; ++db) *(u32x2*)(op + 16 * db) = u32x2{cvtpk(o[b][db][0] * il, o[b][db][1] * il), cvtpk(o[b][db][2] * il, o[b][db][3] * il)};
    }
  }
}

extern "C" __global__ __launch_bounds__(256, 1) void attn_main(const void* q, const void* qk, const void* ks, const void* kz, const void* qv, const void* vs, const void* vz,
    const void* qc, const void* qs, const void* kc, const void* ksn, void* out, void* ws) {
  const unsigned wave = __builtin_amdgcn_readfirstlane(threadIdx.x >> 6);
  const unsigned wg = blockIdx.x;
  constexpr unsigned XM = 1;
  const unsigned x = wg & 7u, idx = wg >> 3;
  const unsigned h = (x >> XM) * (4u << XM) + (idx & ((4u << XM) - 1u)), j = (x & ((1u << XM) - 1u)) * (8u >> XM) + (idx >> (2u + XM));
  auto karg = __builtin_amdgcn_kernarg_segment_ptr();
  unsigned bad;
  asm volatile(
    "s_load_dwordx16 s[40:55], %[karg], 0x0\n"
    "s_load_dwordx8 s[56:63], %[karg], 0x40\n"
    "s_load_dwordx2 s[64:65], %[karg], 0x60\n"
    "s_mov_b32 s36, %[wg]\n"
    "s_mov_b32 s37, %[wave]\n"
    "v_mbcnt_lo_u32_b32 v4, -1, 0\n"
    "v_mbcnt_hi_u32_b32 v4, -1, v4\n"
    "v_lshlrev_b32_e32 v13, 7, v4\n"
    "s_cmp_lg_u32 s37, 3\n"
    "s_cbranch_scc1 .Lattn_nopf\n"
    "s_getpc_b64 s[66:67]\n"
    ".Lattn_pc:\n"
    "s_bfe_u32 s98, s36, 0x30003\n"
    "s_mul_i32 s98, s98, 6144\n"
    "v_add_u32_e32 v7, s98, v13\n"
    "v_min_u32_e32 v7, .Lattn_end-.Lattn_pc-4, v7\n"
    "global_load_dword v23, v7, s[66:67]\n"
    ".Lattn_nopf:\n"
    "s_mov_b32 s38, %[h]\n"
    "s_mov_b32 s39, %[j]\n"
    "v_lshrrev_b32_e32 v6, 4, v4\n"
    "v_lshrrev_b32_e32 v14, 3, v4\n"
    "v_and_b32_e32 v15, 7, v4\n"
    "v_bfe_u32 v12, v4, 1, 3\n"
    "v_and_b32_e32 v8, 1, v4\n"
    "v_lshl_add_u32 v12, v8, 3, v12\n"
    "s_lshl_b32 s66, s37, 11\n"
    "v_lshlrev_b32_e32 v7, 5, v12\n"
    "v_lshl_add_u32 v7, v6, 9, v7\n"
    "v_add_u32_e32 v128, s66, v7\n"
    "v_lshrrev_b32_e32 v129, 1, v128\n"
    "s_lshr_b32 s66, s37, 1\n"
    "s_lshl_b32 s66, s66, 12\n"
    "s_and_b32 s67, s37, 1\n"
    "s_lshl_b32 s67, s67, 8\n"
    "s_add_u32 s66, s66, s67\n"
    "v_lshlrev_b32_e32 v7, 4, v15\n"
    "v_lshl_add_u32 v7, v14, 9, v7\n"
    "v_add_u32_e32 v130, s66, v7\n"
    "s_lshl_b32 s66, s37, 9\n"
    "v_lshlrev_b32_e32 v11, 3, v4\n"
    "v_add_u32_e32 v11, s66, v11\n"
    "s_mov_b32 s74, 0x20000\n"
    "s_mov_b32 s75, 0x20000\n"
    "s_mov_b32 s78, 0x20000\n"
    "s_mov_b32 s79, 0x20000\n"
    "s_mov_b32 s82, 0x10000\n"
    "s_mov_b32 s83, 0x20000\n"
    "s_mov_b32 s86, 0x10000\n"
    "s_mov_b32 s87, 0x20000\n"
    "s_mov_b32 s90, 0x800\n"
    "s_mov_b32 s91, 0x20000\n"
    "s_mov_b32 s70, 0x100000\n"
    "s_mov_b32 s71, 0x20000\n"
    "s_lshl_b32 s66, s38, 3\n"
    "s_add_u32 s66, s66, s39\n"
    "s_lshl_b32 s67, s66, 11\n"
    "v_add_u32_e32 v7, s67, v11\n"
    "s_lshl_b32 s67, s66, 17\n"
    "s_lshl_b32 s98, s39, 16\n"
    "s_waitcnt lgkmcnt(0)\n"
    "global_load_dwordx2 v[28:29], v7, s[44:45]\n"
    "global_load_dwordx2 v[30:31], v7, s[46:47]\n"
    "global_load_dwordx2 v[32:33], v7, s[50:51]\n"
    "global_load_dwordx2 v[34:35], v7, s[52:53]\n"
    "s_add_u32 s72, s42, s67\n"
    "s_addc_u32 s73, s43, 0\n"
    "s_and_b32 s73, s73, 0xffff\n"
    "s_add_u32 s80, s58, s98\n"
    "s_addc_u32 s81, s59, 0\n"
    "s_and_b32 s81, s81, 0xffff\n"
    "s_add_u32 s84, s60, s98\n"
    "s_addc_u32 s85, s61, 0\n"
    "s_and_b32 s85, s85, 0xffff\n"
    "s_add_u32 s76, s48, s67\n"
    "s_addc_u32 s77, s49, 0\n"
    "s_and_b32 s77, s77, 0xffff\n"
    "s_mov_b32 s42, 0\n"
    "s_mov_b32 s43, 0\n"
    "s_mov_b32 s48, 0\n"
    "s_mov_b32 s49, 0\n"
    "s_mov_b32 s58, 0\n"
    "buffer_load_dwordx4 v[140:143], v128, s[72:75], s42 offen\n"
    "buffer_load_dwordx4 v[144:147], v128, s[72:75], s42 offen offset:16\n"
    "s_add_u32 s42, s42, 0x2000\n"
    "s_min_u32 s42, s42, 0x1e000\n"
    "buffer_load_dwordx4 v[148:151], v129, s[80:83], s48 offen\n"
    "buffer_load_dwordx4 v[152:155], v129, s[84:87], s48 offen\n"
    "s_add_u32 s48, s48, 0x1000\n"
    "s_min_u32 s48, s48, 0xf000\n"
    "buffer_load_dwordx4 v[164:167], v130, s[76:79], s43 offen\n"
    "buffer_load_dwordx4 v[168:171], v130, s[76:79], s43 offen offset:128\n"
    "s_add_u32 s43, s43, 0x2000\n"
    "s_lshl_b32 s67, s38, 11\n"
    "s_add_u32 s67, s67, 0x2800000\n"
    "s_add_u32 s88, s64, s67\n"
    "s_addc_u32 s89, s65, 0\n"
    "s_and_b32 s89, s89, 0xffff\n"
    "v_mov_b32_e32 v7, 0x400\n"
    "buffer_load_dword v63, v7, s[88:91], 0 offen sc1\n"
    "s_mul_i32 s67, s38, 0x108800\n"
    "s_add_u32 s68, s64, s67\n"
    "s_addc_u32 s69, s65, 0\n"
    "s_and_b32 s69, s69, 0xffff\n"
    "s_and_b32 s59, s38, 7\n"
    "s_lshl_b32 s59, s59, 2\n"
    "s_add_u32 s59, s59, s37\n"
    "s_bitcmp1_b32 s59, 4\n"
    "s_cselect_b32 s46, s56, s54\n"
    "s_cselect_b32 s47, s57, s55\n"
    "s_and_b32 s59, s59, 15\n"
    "s_lshl_b32 s59, s59, 12\n"
    "s_add_u32 s59, s59, s98\n"
    "s_add_u32 s46, s46, s59\n"
    "s_addc_u32 s47, s47, 0\n"
    "s_lshl_b32 s67, s37, 14\n"
    "s_add_u32 s98, s98, s67\n"
    "s_add_u32 s54, s54, s98\n"
    "s_addc_u32 s55, s55, 0\n"
    "s_add_u32 s56, s56, s98\n"
    "s_addc_u32 s57, s57, 0\n"
    "s_lshl_b32 s98, s66, 16\n"
    "s_add_u32 s98, s98, s67\n"
    "s_add_u32 s40, s40, s98\n"
    "s_addc_u32 s41, s41, 0\n"
    "s_add_u32 s62, s62, s98\n"
    "s_addc_u32 s63, s63, 0\n"
    "v_lshlrev_b32_e32 v9, 4, v15\n"
    "v_lshl_add_u32 v9, v14, 8, v9\n"
    "v_add_u32_e32 v10, 0x3000, v9\n"
    "v_add_u32_e32 v9, 0x1000, v9\n"
    "v_and_b32_e32 v58, 0xf80, v13\n"
    "global_load_dword v58, v58, s[46:47]\n"
    "s_lshl_b32 s92, s37, 12\n"
    "s_mov_b32 s95, 0xffff0000\n"
    "s_mov_b32 s97, 0xea60\n"
    "s_mov_b32 s50, 0x05040100\n"
    "s_mov_b32 s51, 0x07060302\n"
    "v_and_b32_e32 v5, 15, v4\n"
    "v_xor_b32_e32 v7, 1, v4\n"
    "v_lshlrev_b32_e32 v137, 31, v7\n"
    "v_lshlrev_b32_e32 v7, 4, v4\n"
    "s_lshl_b32 s67, s37, 10\n"
    "s_add_u32 s67, s67, 0x24000\n"
    "v_add_u32_e32 v135, s67, v7\n"
    "s_lshr_b32 s98, s37, 1\n"
    "s_mul_i32 s98, s98, 0x8000\n"
    "s_lshl_b32 s59, s39, 11\n"
    "s_add_u32 s59, s59, s98\n"
    "s_and_b32 s66, s37, 1\n"
    "s_lshl_b32 s66, s66, 10\n"
    "s_add_u32 s59, s59, s66\n"
    "s_add_u32 s59, s59, 0x4000\n"
    "s_lshr_b32 s66, s66, 4\n"
    "v_lshrrev_b32_e32 v8, 4, v4\n"
    "v_lshl_add_u32 v8, v8, 4, s66\n"
    "v_xor_b32_e32 v8, v8, v7\n"
    "v_add_u32_e32 v132, s59, v8\n"
    "v_bfe_u32 v7, v4, 2, 2\n"
    "v_lshlrev_b32_e32 v7, 7, v7\n"
    "v_and_b32_e32 v8, 3, v4\n"
    "v_lshl_add_u32 v7, v8, 4, v7\n"
    "v_and_b32_e32 v8, 1, v6\n"
    "v_lshl_add_u32 v7, v8, 6, v7\n"
    "v_lshrrev_b32_e32 v8, 1, v6\n"
    "v_lshl_add_u32 v7, v8, 3, v7\n"
    "v_add_u32_e32 v136, s67, v7\n"
    "s_and_b32 s66, s39, 3\n"
    "s_lshl_b32 s66, s66, 2\n"
    "v_add_u32_e32 v7, s66, v6\n"
    "v_xor_b32_e32 v8, v12, v7\n"
    "v_lshlrev_b32_e32 v8, 4, v8\n"
    "v_lshl_add_u32 v8, v7, 8, v8\n"
    "s_lshr_b32 s66, s39, 2\n"
    "s_lshl_b32 s66, s66, 13\n"
    "s_add_u32 s98, s98, s66\n"
    "s_and_b32 s66, s37, 1\n"
    "s_lshl_b32 s66, s66, 12\n"
    "s_add_u32 s98, s98, s66\n"
    "v_add_u32_e32 v131, s98, v8\n"
    "s_lshl_b32 s66, s37, 7\n"
    "s_add_u32 s66, s66, 0x20000\n"
    "v_lshrrev_b32_e32 v7, 2, v12\n"
    "v_lshlrev_b32_e32 v7, 3, v7\n"
    "v_lshl_add_u32 v7, v6, 5, v7\n"
    "v_add_u32_e32 v133, s66, v7\n"
    "s_lshr_b32 s66, s37, 1\n"
    "s_lshl_b32 s66, s66, 8\n"
    "s_and_b32 s67, s37, 1\n"
    "s_lshl_b32 s67, s67, 4\n"
    "s_add_u32 s66, s66, s67\n"
    "s_add_u32 s66, s66, 0x22000\n"
    "v_lshlrev_b32_e32 v7, 5, v14\n"
    "v_add_u32_e32 v134, s66, v7\n"
    "v_mov_b32_e32 v59, v11\n"
    "v_lshlrev_b32_e32 v11, 2, v11\n"
    "v_add_u32_e32 v11, 0x20000, v11\n"
    "v_mov_b32_e32 v7, 0x25000\n"
    "v_mov_b32_e32 v8, 0\n"
    "ds_write_b32 v7, v8\n"
    "buffer_load_dwordx4 v[240:243], v128, s[72:75], s42 offen\n"
    "buffer_load_dwordx4 v[244:247], v128, s[72:75], s42 offen offset:16\n"
    "s_add_u32 s42, s42, 0x2000\n"
    "s_min_u32 s42, s42, 0x1e000\n"
    "buffer_load_dwordx4 v[248:251], v129, s[80:83], s48 offen\n"
    "buffer_load_dwordx4 v[252:255], v129, s[84:87], s48 offen\n"
    "s_add_u32 s48, s48, 0x1000\n"
    "s_min_u32 s48, s48, 0xf000\n"
    "s_waitcnt vmcnt(12)\n"
    "v_lshlrev_b32_e32 v44, 16, v32\n"
    "v_lshlrev_b32_e32 v45, 16, v34\n"
    "v_and_b32_e32 v46, s95, v32\n"
    "v_and_b32_e32 v47, s95, v34\n"
    "v_lshlrev_b32_e32 v48, 16, v33\n"
    "v_lshlrev_b32_e32 v49, 16, v35\n"
    "v_and_b32_e32 v50, s95, v33\n"
    "v_and_b32_e32 v51, s95, v35\n"
    "v_mul_f32_e64 v45, -v45, v44\n"
    "v_mul_f32_e64 v47, -v47, v46\n"
    "v_mul_f32_e64 v49, -v49, v48\n"
    "v_mul_f32_e64 v51, -v51, v50\n"
    "v_add_f32_e64 v7, abs(v44), abs(v46)\n"
    "v_add_f32_e64 v8, abs(v45), abs(v47)\n"
    "v_add_f32_e64 v7, v7, abs(v48)\n"
    "v_add_f32_e64 v8, v8, abs(v49)\n"
    "v_add_f32_e64 v7, v7, abs(v50)\n"
    "v_add_f32_e64 v8, v8, abs(v51)\n"
    "v_fmac_f32_e32 v8, 0x4f000000, v7\n"
    "v_cmp_nge_f32_e32 vcc, 0x54800000, v8\n"
    "v_mov_b32_e32 v7, 0x7fc00000\n"
    "ds_write_b128 v11, v[44:47] offset:8192\n"
    "ds_write_b128 v11, v[48:51] offset:8208\n"
    "v_lshlrev_b32_e32 v36, 16, v28\n"
    "v_lshlrev_b32_e32 v37, 16, v30\n"
    "v_and_b32_e32 v38, s95, v28\n"
    "v_and_b32_e32 v39, s95, v30\n"
    "v_lshlrev_b32_e32 v40, 16, v29\n"
    "v_lshlrev_b32_e32 v41, 16, v31\n"
    "v_and_b32_e32 v42, s95, v29\n"
    "v_and_b32_e32 v43, s95, v31\n"
    "v_mul_f32_e32 v36, 0x3e0293ee, v36\n"
    "v_mul_f32_e64 v37, -v37, v36\n"
    "v_mul_f32_e32 v38, 0x3e0293ee, v38\n"
    "v_mul_f32_e64 v39, -v39, v38\n"
    "v_mul_f32_e32 v40, 0x3e0293ee, v40\n"
    "v_mul_f32_e64 v41, -v41, v40\n"
    "v_mul_f32_e32 v42, 0x3e0293ee, v42\n"
    "v_mul_f32_e64 v43, -v43, v42\n"
    "v_cndmask_b32_e32 v36, v36, v7, vcc\n"
    "ds_write_b128 v11, v[36:39] offset:0\n"
    "ds_write_b128 v11, v[40:43] offset:16\n"
    "s_waitcnt lgkmcnt(0)\n"
    "s_barrier\n"
    "ds_read_b64 v[138:139], v133\n"
    "v_add_u32_e32 v133, 0x200, v133\n"
    "s_waitcnt vmcnt(8)\n"
    "v_cvt_f32_i32_e32 v140, v140\n"
    "v_cvt_f32_i32_e32 v141, v141\n"
    "v_cvt_f32_i32_e32 v142, v142\n"
    "v_cvt_f32_i32_e32 v143, v143\n"
    "v_cvt_f32_i32_e32 v144, v144\n"
    "v_cvt_f32_i32_e32 v145, v145\n"
    "v_cvt_f32_i32_e32 v146, v146\n"
    "v_cvt_f32_i32_e32 v147, v147\n"
    "s_waitcnt lgkmcnt(0)\n"
    "v_fma_f32 v140, v140, v138, v139\n"
    "v_fma_f32 v141, v141, v138, v139\n"
    "v_fma_f32 v142, v142, v138, v139\n"
    "v_fma_f32 v143, v143, v138, v139\n"
    "v_fma_f32 v144, v144, v138, v139\n"
    "v_fma_f32 v145, v145, v138, v139\n"
    "v_fma_f32 v146, v146, v138, v139\n"
    "v_fma_f32 v147, v147, v138, v139\n"
    "v_lshlrev_b32_e32 v156, 16, v148\n"
    "v_and_b32_e32 v157, s95, v148\n"
    "v_lshl_add_u32 v158, v152, 16, v137\n"
    "v_bitop3_b32 v159, v152, s95, v137 bitop3:0x6a\n"
    "v_mul_f32_e32 v156, v140, v156\n"
    "v_mul_f32_e32 v157, v141, v157\n"
    "v_fmac_f32_dpp v156, v140, v158 quad_perm:[1,0,3,2] row_mask:0xf bank_mask:0xf\n"
    "v_fmac_f32_dpp v157, v141, v159 quad_perm:[1,0,3,2] row_mask:0xf bank_mask:0xf\n"
    "v_cvt_pk_bf16_f32 v160, v156, v157\n"
    "v_lshlrev_b32_e32 v156, 16, v149\n"
    "v_and_b32_e32 v157, s95, v149\n"
    "v_lshl_add_u32 v158, v153, 16, v137\n"
    "v_bitop3_b32 v159, v153, s95, v137 bitop3:0x6a\n"
    "v_mul_f32_e32 v156, v142, v156\n"
    "v_mul_f32_e32 v157, v143, v157\n"
    "v_fmac_f32_dpp v156, v142, v158 quad_perm:[1,0,3,2] row_mask:0xf bank_mask:0xf\n"
    "v_fmac_f32_dpp v157, v143, v159 quad_perm:[1,0,3,2] row_mask:0xf bank_mask:0xf\n"
    "v_cvt_pk_bf16_f32 v161, v156, v157\n"
    "v_lshlrev_b32_e32 v156, 16, v150\n"
    "v_and_b32_e32 v157, s95, v150\n"
    "v_lshl_add_u32 v158, v154, 16, v137\n"
    "v_bitop3_b32 v159, v154, s95, v137 bitop3:0x6a\n"
    "v_mul_f32_e32 v156, v144, v156\n"
    "v_mul_f32_e32 v157, v145, v157\n"
    "v_fmac_f32_dpp v156, v144, v158 quad_perm:[1,0,3,2] row_mask:0xf bank_mask:0xf\n"
    "v_fmac_f32_dpp v157, v145, v159 quad_perm:[1,0,3,2] row_mask:0xf bank_mask:0xf\n"
    "v_cvt_pk_bf16_f32 v162, v156, v157\n"
    "v_lshlrev_b32_e32 v156, 16, v151\n"
    "v_and_b32_e32 v157, s95, v151\n"
    "v_lshl_add_u32 v158, v155, 16, v137\n"
    "v_bitop3_b32 v159, v155, s95, v137 bitop3:0x6a\n"
    "v_mul_f32_e32 v156, v146, v156\n"
    "v_mul_f32_e32 v157, v147, v157\n"
    "v_fmac_f32_dpp v156, v146, v158 quad_perm:[1,0,3,2] row_mask:0xf bank_mask:0xf\n"
    "v_fmac_f32_dpp v157, v147, v159 quad_perm:[1,0,3,2] row_mask:0xf bank_mask:0xf\n"
    "v_cvt_pk_bf16_f32 v163, v156, v157\n"
    "buffer_store_dwordx4 v[160:163], v131, s[68:71], s49 offen sc1\n"
    "s_add_u32 s49, s49, 0x10000\n"
    "s_nop 0\n"
    "global_load_dwordx4 v[64:67], v9, s[40:41] offset:-4096\n"
    "global_load_dwordx4 v[68:71], v9, s[40:41] offset:-3968\n"
    "global_load_dwordx4 v[96:99], v9, s[40:41] offset:-2048\n"
    "global_load_dwordx4 v[100:103], v9, s[40:41] offset:-1920\n"
    "global_load_dwordx4 v[72:75], v9, s[40:41] offset:0\n"
    "global_load_dwordx4 v[76:79], v9, s[40:41] offset:128\n"
    "ds_read_b128 v[180:183], v134\n"
    "v_add_u32_e32 v134, 0x200, v134\n"
    "s_waitcnt vmcnt(13)\n"
    "v_cvt_f32_i32_e32 v164, v164\n"
    "v_cvt_f32_i32_e32 v165, v165\n"
    "v_cvt_f32_i32_e32 v166, v166\n"
    "v_cvt_f32_i32_e32 v167, v167\n"
    "v_cvt_f32_i32_e32 v168, v168\n"
    "v_cvt_f32_i32_e32 v169, v169\n"
    "v_cvt_f32_i32_e32 v170, v170\n"
    "v_cvt_f32_i32_e32 v171, v171\n"
    "s_waitcnt lgkmcnt(0)\n"
    "v_fma_f32 v164, v164, v180, v181\n"
    "v_fma_f32 v165, v165, v180, v181\n"
    "v_fma_f32 v166, v166, v180, v181\n"
    "v_fma_f32 v167, v167, v180, v181\n"
    "v_fma_f32 v168, v168, v182, v183\n"
    "v_fma_f32 v169, v169, v182, v183\n"
    "v_fma_f32 v170, v170, v182, v183\n"
    "v_fma_f32 v171, v171, v182, v183\n"
    "v_cvt_pk_bf16_f32 v184, v164, v165\n"
    "v_cvt_pk_bf16_f32 v185, v166, v167\n"
    "v_cvt_pk_bf16_f32 v186, v168, v169\n"
    "v_cvt_pk_bf16_f32 v187, v170, v171\n"
    "ds_write_b128 v135, v[184:187]\n"
    "ds_read_b64_tr_b16 v[188:189], v136\n"
    "ds_read_b64_tr_b16 v[190:191], v136 offset:512\n"
    "s_waitcnt lgkmcnt(0)\n"
    "buffer_store_dwordx4 v[188:191], v132, s[68:71], s58 offen sc1\n"
    "s_add_u32 s58, s58, 0x10000\n"
    "s_nop 0\n"
    "ds_read_b64 v[138:139], v133\n"
    "v_add_u32_e32 v133, 0x200, v133\n"
    "s_waitcnt vmcnt(8)\n"
    "v_cvt_f32_i32_e32 v240, v240\n"
    "v_cvt_f32_i32_e32 v241, v241\n"
    "v_cvt_f32_i32_e32 v242, v242\n"
    "v_cvt_f32_i32_e32 v243, v243\n"
    "v_cvt_f32_i32_e32 v244, v244\n"
    "v_cvt_f32_i32_e32 v245, v245\n"
    "v_cvt_f32_i32_e32 v246, v246\n"
    "v_cvt_f32_i32_e32 v247, v247\n"
    "s_waitcnt lgkmcnt(0)\n"
    "v_fma_f32 v240, v240, v138, v139\n"
    "v_fma_f32 v241, v241, v138, v139\n"
    "v_fma_f32 v242, v242, v138, v139\n"
    "v_fma_f32 v243, v243, v138, v139\n"
    "v_fma_f32 v244, v244, v138, v139\n"
    "v_fma_f32 v245, v245, v138, v139\n"
    "v_fma_f32 v246, v246, v138, v139\n"
    "v_fma_f32 v247, v247, v138, v139\n"
    "v_lshlrev_b32_e32 v156, 16, v248\n"
    "v_and_b32_e32 v157, s95, v248\n"
    "v_lshl_add_u32 v158, v252, 16, v137\n"
    "v_bitop3_b32 v159, v252, s95, v137 bitop3:0x6a\n"
    "v_mul_f32_e32 v156, v240, v156\n"
    "v_mul_f32_e32 v157, v241, v157\n"
    "v_fmac_f32_dpp v156, v240, v158 quad_perm:[1,0,3,2] row_mask:0xf bank_mask:0xf\n"
    "v_fmac_f32_dpp v157, v241, v159 quad_perm:[1,0,3,2] row_mask:0xf bank_mask:0xf\n"
    "v_cvt_pk_bf16_f32 v160, v156, v157\n"
    "v_lshlrev_b32_e32 v156, 16, v249\n"
    "v_and_b32_e32 v157, s95, v249\n"
    "v_lshl_add_u32 v158, v253, 16, v137\n"
    "v_bitop3_b32 v159, v253, s95, v137 bitop3:0x6a\n"
    "v_mul_f32_e32 v156, v242, v156\n"
    "v_mul_f32_e32 v157, v243, v157\n"
    "v_fmac_f32_dpp v156, v242, v158 quad_perm:[1,0,3,2] row_mask:0xf bank_mask:0xf\n"
    "v_fmac_f32_dpp v157, v243, v159 quad_perm:[1,0,3,2] row_mask:0xf bank_mask:0xf\n"
    "v_cvt_pk_bf16_f32 v161, v156, v157\n"
    "v_lshlrev_b32_e32 v156, 16, v250\n"
    "v_and_b32_e32 v157, s95, v250\n"
    "v_lshl_add_u32 v158, v254, 16, v137\n"
    "v_bitop3_b32 v159, v254, s95, v137 bitop3:0x6a\n"
    "v_mul_f32_e32 v156, v244, v156\n"
    "v_mul_f32_e32 v157, v245, v157\n"
    "v_fmac_f32_dpp v156, v244, v158 quad_perm:[1,0,3,2] row_mask:0xf bank_mask:0xf\n"
    "v_fmac_f32_dpp v157, v245, v159 quad_perm:[1,0,3,2] row_mask:0xf bank_mask:0xf\n"
    "v_cvt_pk_bf16_f32 v162, v156, v157\n"
    "v_lshlrev_b32_e32 v156, 16, v251\n"
    "v_and_b32_e32 v157, s95, v251\n"
    "v_lshl_add_u32 v158, v255, 16, v137\n"
    "v_bitop3_b32 v159, v255, s95, v137 bitop3:0x6a\n"
    "v_mul_f32_e32 v156, v246, v156\n"
    "v_mul_f32_e32 v157, v247, v157\n"
    "v_fmac_f32_dpp v156, v246, v158 quad_perm:[1,0,3,2] row_mask:0xf bank_mask:0xf\n"
    "v_fmac_f32_dpp v157, v247, v159 quad_perm:[1,0,3,2] row_mask:0xf bank_mask:0xf\n"
    "v_cvt_pk_bf16_f32 v163, v156, v157\n"
    "buffer_store_dwordx4 v[160:163], v131, s[68:71], s49 offen sc1\n"
    "s_add_u32 s49, s49, 0x10000\n"
    "s_nop 0\n"
    "global_load_dwordx4 v[192:195], v9, s[54:55] offset:-4096\n"
    "global_load_dwordx4 v[196:199], v9, s[54:55] offset:-3968\n"
    "global_load_dwordx4 v[200:203], v9, s[56:57] offset:-4096\n"
    "global_load_dwordx4 v[204:207], v9, s[56:57] offset:-3968\n"
    "global_load_dwordx4 v[104:107], v9, s[40:41] offset:2048\n"
    "global_load_dwordx4 v[108:111], v9, s[40:41] offset:2176\n"
    "v_xor_b32_e32 v7, v5, v6\n"
    "v_xor_b32_e32 v8, 0, v7\n"
    "v_lshlrev_b32_e32 v8, 4, v8\n"
    "v_lshl_add_u32 v16, v5, 8, v8\n"
    "v_xor_b32_e32 v8, 4, v7\n"
    "v_lshlrev_b32_e32 v8, 4, v8\n"
    "v_lshl_add_u32 v17, v5, 8, v8\n"
    "v_xor_b32_e32 v8, 8, v7\n"
    "v_lshlrev_b32_e32 v8, 4, v8\n"
    "v_lshl_add_u32 v18, v5, 8, v8\n"
    "v_xor_b32_e32 v8, 12, v7\n"
    "v_lshlrev_b32_e32 v8, 4, v8\n"
    "v_lshl_add_u32 v19, v5, 8, v8\n"
    "v_lshlrev_b32_e32 v7, 11, v6\n"
    "v_lshl_add_u32 v7, v5, 7, v7\n"
    "v_lshrrev_b32_e32 v8, 1, v5\n"
    "v_lshl_add_u32 v7, v8, 4, v7\n"
    "v_add_u32_e32 v20, 0x10000, v7\n"
    "v_lshlrev_b32_e32 v7, 4, v4\n"
    "v_add_u32_e32 v21, s92, v7\n"
    "v_add_u32_e32 v22, 0x4000, v21\n"
    "s_lshl_b32 s66, s39, 7\n"
    "v_mov_b32_e32 v7, 0x7fffff00\n"
    "v_mov_b32_e32 v8, s66\n"
    "v_cmp_eq_u32_e32 vcc, 0x200, v59\n"
    "s_nop 1\n"
    "v_cndmask_b32_e32 v60, v7, v8, vcc\n"
    "v_cmp_gt_u32_e32 vcc, 64, v59\n"
    "v_lshlrev_b32_e32 v8, 4, v59\n"
    "s_nop 0\n"
    "v_cndmask_b32_e32 v61, v7, v8, vcc\n"
    "v_mov_b32_e32 v24, 0x3f803f80\n"
    "v_mov_b32_e32 v25, 0x3f803f80\n"
    "v_mov_b32_e32 v26, 0x3f803f80\n"
    "v_mov_b32_e32 v27, 0x3f803f80\n"
    "s_lshl_b32 s98, s37, 13\n"
    "s_add_u32 s98, s98, 0x18000\n"
    "v_xor_b32_e32 v5, v15, v14\n"
    "v_lshlrev_b32_e32 v5, 4, v5\n"
    "v_lshl_add_u32 v5, v14, 8, v5\n"
    "v_add_u32_e32 v5, s98, v5\n"
    "v_xor_b32_e32 v6, v15, v14\n"
    "v_xor_b32_e32 v6, 8, v6\n"
    "v_lshlrev_b32_e32 v6, 4, v6\n"
    "v_lshl_add_u32 v6, v14, 8, v6\n"
    "v_add_u32_e32 v6, s98, v6\n"
    "v_add_u32_e32 v52, s98, v16\n"
    "v_add_u32_e32 v53, s98, v17\n"
    "v_add_u32_e32 v54, s98, v18\n"
    "v_add_u32_e32 v55, s98, v19\n"
    "v_accvgpr_write_b32 a224, 0\n"
    "v_accvgpr_write_b32 a225, 0\n"
    "v_accvgpr_write_b32 a226, 0\n"
    "v_accvgpr_write_b32 a227, 0\n"
    "s_nop 1\n"
    "v_mfma_f32_32x32x16_bf16 a[0:15], a[224:227], a[224:227], 0\n"
    "v_mfma_f32_32x32x16_bf16 a[16:31], a[224:227], a[224:227], 0\n"
    "v_mfma_f32_32x32x16_bf16 a[32:47], a[224:227], a[224:227], 0\n"
    "v_mfma_f32_32x32x16_bf16 a[48:63], a[224:227], a[224:227], 0\n"
    "v_mfma_f32_32x32x16_bf16 a[64:79], a[224:227], a[224:227], 0\n"
    "v_mfma_f32_32x32x16_bf16 a[80:95], a[224:227], a[224:227], 0\n"
    "v_mfma_f32_32x32x16_bf16 a[96:111], a[224:227], a[224:227], 0\n"
    "v_mfma_f32_32x32x16_bf16 a[112:127], a[224:227], a[224:227], 0\n"
    "global_load_dwordx4 v[208:211], v9, s[54:55] offset:-2048\n"
    "global_load_dwordx4 v[212:215], v9, s[54:55] offset:-1920\n"
    "global_load_dwordx4 v[216:219], v9, s[56:57] offset:-2048\n"
    "global_load_dwordx4 v[220:223], v9, s[56:57] offset:-1920\n"
    "global_load_dwordx4 v[224:227], v9, s[54:55] offset:0\n"
    "global_load_dwordx4 v[228:231], v9, s[54:55] offset:128\n"
    "global_load_dwordx4 v[232:235], v9, s[56:57] offset:0\n"
    "global_load_dwordx4 v[236:239], v9, s[56:57] offset:128\n"
    "s_waitcnt vmcnt(10)\n"
    "v_perm_b32 v28, v68, v64, s50\n"
    "v_perm_b32 v29, v200, v192, s50\n"
    "v_perm_b32 v30, v196, v204, s50\n"
    "v_perm_b32 v31, v68, v64, s51\n"
    "v_perm_b32 v32, v200, v192, s51\n"
    "v_perm_b32 v33, v196, v204, s51\n"
    "v_dot2_f32_bf16 v29, v28, v29, 0 neg_hi:[0,1,0]\n"
    "v_dot2_f32_bf16 v30, v28, v30, 0\n"
    "v_dot2_f32_bf16 v32, v31, v32, 0 neg_hi:[0,1,0]\n"
    "v_dot2_f32_bf16 v33, v31, v33, 0\n"
    "v_perm_b32 v34, v69, v65, s50\n"
    "v_perm_b32 v35, v201, v193, s50\n"
    "v_perm_b32 v36, v197, v205, s50\n"
    "v_perm_b32 v37, v69, v65, s51\n"
    "v_perm_b32 v38, v201, v193, s51\n"
    "v_perm_b32 v39, v197, v205, s51\n"
    "v_cvt_pk_bf16_f32 v44, v29, v32\n"
    "v_cvt_pk_bf16_f32 v48, v30, v33\n"
    "v_dot2_f32_bf16 v35, v34, v35, 0 neg_hi:[0,1,0]\n"
    "v_dot2_f32_bf16 v36, v34, v36, 0\n"
    "v_dot2_f32_bf16 v38, v37, v38, 0 neg_hi:[0,1,0]\n"
    "v_dot2_f32_bf16 v39, v37, v39, 0\n"
    "v_perm_b32 v28, v70, v66, s50\n"
    "v_perm_b32 v29, v202, v194, s50\n"
    "v_perm_b32 v30, v198, v206, s50\n"
    "v_perm_b32 v31, v70, v66, s51\n"
    "v_perm_b32 v32, v202, v194, s51\n"
    "v_perm_b32 v33, v198, v206, s51\n"
    "v_cvt_pk_bf16_f32 v45, v35, v38\n"
    "v_cvt_pk_bf16_f32 v49, v36, v39\n"
    "v_dot2_f32_bf16 v29, v28, v29, 0 neg_hi:[0,1,0]\n"
    "v_dot2_f32_bf16 v30, v28, v30, 0\n"
    "v_dot2_f32_bf16 v32, v31, v32, 0 neg_hi:[0,1,0]\n"
    "v_dot2_f32_bf16 v33, v31, v33, 0\n"
    "v_perm_b32 v34, v71, v67, s50\n"
    "v_perm_b32 v35, v203, v195, s50\n"
    "v_perm_b32 v36, v199, v207, s50\n"
    "v_perm_b32 v37, v71, v67, s51\n"
    "v_perm_b32 v38, v203, v195, s51\n"
    "v_perm_b32 v39, v199, v207, s51\n"
    "v_cvt_pk_bf16_f32 v46, v29, v32\n"
    "v_cvt_pk_bf16_f32 v50, v30, v33\n"
    "v_dot2_f32_bf16 v35, v34, v35, 0 neg_hi:[0,1,0]\n"
    "v_dot2_f32_bf16 v36, v34, v36, 0\n"
    "v_dot2_f32_bf16 v38, v37, v38, 0 neg_hi:[0,1,0]\n"
    "v_dot2_f32_bf16 v39, v37, v39, 0\n"
    "s_nop 1\n"
    "v_cvt_pk_bf16_f32 v47, v35, v38\n"
    "v_cvt_pk_bf16_f32 v51, v36, v39\n"
    "ds_write_b128 v5, v[44:47] offset:0\n"
    "ds_write_b128 v6, v[48:51] offset:0\n"
    "s_waitcnt vmcnt(14)\n"
    "s_barrier\n"
    "v_add_u32_e32 v63, 1, v63\n"
    "v_mov_b32_e32 v62, 61\n"
    "buffer_store_dwordx2 v[62:63], v60, s[88:91], 0 offen sc1\n"
    "buffer_load_dwordx4 v[172:175], v130, s[76:79], s43 offen\n"
    "buffer_load_dwordx4 v[176:179], v130, s[76:79], s43 offen offset:128\n"
    "s_add_u32 s43, s43, 0x2000\n"
    "global_load_dwordx4 v[80:83], v10, s[40:41] offset:-4096\n"
    "global_load_dwordx4 v[84:87], v10, s[40:41] offset:-3968\n"
    "global_load_dwordx4 v[192:195], v9, s[54:55] offset:2048\n"
    "global_load_dwordx4 v[196:199], v9, s[54:55] offset:2176\n"
    "global_load_dwordx4 v[200:203], v9, s[56:57] offset:2048\n"
    "global_load_dwordx4 v[204:207], v9, s[56:57] offset:2176\n"
    "s_waitcnt vmcnt(13)\n"
    "v_perm_b32 v28, v100, v96, s50\n"
    "v_perm_b32 v29, v216, v208, s50\n"
    "v_perm_b32 v30, v212, v220, s50\n"
    "v_perm_b32 v31, v100, v96, s51\n"
    "v_perm_b32 v32, v216, v208, s51\n"
    "v_perm_b32 v33, v212, v220, s51\n"
    "v_dot2_f32_bf16 v29, v28, v29, 0 neg_hi:[0,1,0]\n"
    "v_dot2_f32_bf16 v30, v28, v30, 0\n"
    "v_dot2_f32_bf16 v32, v31, v32, 0 neg_hi:[0,1,0]\n"
    "v_dot2_f32_bf16 v33, v31, v33, 0\n"
    "v_perm_b32 v34, v101, v97, s50\n"
    "v_perm_b32 v35, v217, v209, s50\n"
    "v_perm_b32 v36, v213, v221, s50\n"
    "v_perm_b32 v37, v101, v97, s51\n"
    "v_perm_b32 v38, v217, v209, s51\n"
    "v_perm_b32 v39, v213, v221, s51\n"
    "v_cvt_pk_bf16_f32 v44, v29, v32\n"
    "v_cvt_pk_bf16_f32 v48, v30, v33\n"
    "v_dot2_f32_bf16 v35, v34, v35, 0 neg_hi:[0,1,0]\n"
    "v_dot2_f32_bf16 v36, v34, v36, 0\n"
    "v_dot2_f32_bf16 v38, v37, v38, 0 neg_hi:[0,1,0]\n"
    "v_dot2_f32_bf16 v39, v37, v39, 0\n"
    "v_perm_b32 v28, v102, v98, s50\n"
    "v_perm_b32 v29, v218, v210, s50\n"
    "v_perm_b32 v30, v214, v222, s50\n"
    "v_perm_b32 v31, v102, v98, s51\n"
    "v_perm_b32 v32, v218, v210, s51\n"
    "v_perm_b32 v33, v214, v222, s51\n"
    "v_cvt_pk_bf16_f32 v45, v35, v38\n"
    "v_cvt_pk_bf16_f32 v49, v36, v39\n"
    "v_dot2_f32_bf16 v29, v28, v29, 0 neg_hi:[0,1,0]\n"
    "v_dot2_f32_bf16 v30, v28, v30, 0\n"
    "v_dot2_f32_bf16 v32, v31, v32, 0 neg_hi:[0,1,0]\n"
    "v_dot2_f32_bf16 v33, v31, v33, 0\n"
    "v_perm_b32 v34, v103, v99, s50\n"
    "v_perm_b32 v35, v219, v211, s50\n"
    "v_perm_b32 v36, v215, v223, s50\n"
    "v_perm_b32 v37, v103, v99, s51\n"
    "v_perm_b32 v38, v219, v211, s51\n"
    "v_perm_b32 v39, v215, v223, s51\n"
    "v_cvt_pk_bf16_f32 v46, v29, v32\n"
    "v_cvt_pk_bf16_f32 v50, v30, v33\n"
    "v_dot2_f32_bf16 v35, v34, v35, 0 neg_hi:[0,1,0]\n"
    "v_dot2_f32_bf16 v36, v34, v36, 0\n"
    "v_dot2_f32_bf16 v38, v37, v38, 0 neg_hi:[0,1,0]\n"
    "v_dot2_f32_bf16 v39, v37, v39, 0\n"
    "s_nop 1\n"
    "v_cvt_pk_bf16_f32 v47, v35, v38\n"
    "v_cvt_pk_bf16_f32 v51, v36, v39\n"
    "ds_write_b128 v6, v[44:47] offset:2048\n"
    "ds_write_b128 v5, v[48:51] offset:2048\n"
    "ds_read_b128 a[128:131], v52 offset:0\n"
    "ds_read_b128 a[132:135], v53 offset:0\n"
    "ds_read_b128 a[136:139], v54 offset:0\n"
    "ds_read_b128 a[140:143], v55 offset:0\n"
    "global_load_dwordx4 v[112:115], v10, s[40:41] offset:-2048\n"
    "global_load_dwordx4 v[116:119], v10, s[40:41] offset:-1920\n"
    "global_load_dwordx4 v[208:211], v10, s[54:55] offset:-4096\n"
    "global_load_dwordx4 v[212:215], v10, s[54:55] offset:-3968\n"
    "global_load_dwordx4 v[216:219], v10, s[56:57] offset:-4096\n"
    "global_load_dwordx4 v[220:223], v10, s[56:57] offset:-3968\n"
    "buffer_load_dwordx2 v[56:57], v61, s[88:91], 0 offen sc1\n"
    "s_waitcnt vmcnt(16)\n"
    "v_perm_b32 v28, v76, v72, s50\n"
    "v_perm_b32 v29, v232, v224, s50\n"
    "v_perm_b32 v30, v228, v236, s50\n"
    "v_perm_b32 v31, v76, v72, s51\n"
    "v_perm_b32 v32, v232, v224, s51\n"
    "v_perm_b32 v33, v228, v236, s51\n"
    "v_dot2_f32_bf16 v29, v28, v29, 0 neg_hi:[0,1,0]\n"
    "v_dot2_f32_bf16 v30, v28, v30, 0\n"
    "v_dot2_f32_bf16 v32, v31, v32, 0 neg_hi:[0,1,0]\n"
    "v_dot2_f32_bf16 v33, v31, v33, 0\n"
    "v_perm_b32 v34, v77, v73, s50\n"
    "v_perm_b32 v35, v233, v225, s50\n"
    "v_perm_b32 v36, v229, v237, s50\n"
    "v_perm_b32 v37, v77, v73, s51\n"
    "v_perm_b32 v38, v233, v225, s51\n"
    "v_perm_b32 v39, v229, v237, s51\n"
    "v_cvt_pk_bf16_f32 v44, v29, v32\n"
    "v_cvt_pk_bf16_f32 v48, v30, v33\n"
    "v_dot2_f32_bf16 v35, v34, v35, 0 neg_hi:[0,1,0]\n"
    "v_dot2_f32_bf16 v36, v34, v36, 0\n"
    "v_dot2_f32_bf16 v38, v37, v38, 0 neg_hi:[0,1,0]\n"
    "v_dot2_f32_bf16 v39, v37, v39, 0\n"
    "v_perm_b32 v28, v78, v74, s50\n"
    "v_perm_b32 v29, v234, v226, s50\n"
    "v_perm_b32 v30, v230, v238, s50\n"
    "v_perm_b32 v31, v78, v74, s51\n"
    "v_perm_b32 v32, v234, v226, s51\n"
    "v_perm_b32 v33, v230, v238, s51\n"
    "v_cvt_pk_bf16_f32 v45, v35, v38\n"
    "v_cvt_pk_bf16_f32 v49, v36, v39\n"
    "v_dot2_f32_bf16 v29, v28, v29, 0 neg_hi:[0,1,0]\n"
    "v_dot2_f32_bf16 v30, v28, v30, 0\n"
    "v_dot2_f32_bf16 v32, v31, v32, 0 neg_hi:[0,1,0]\n"
    "v_dot2_f32_bf16 v33, v31, v33, 0\n"
    "v_perm_b32 v34, v79, v75, s50\n"
    "v_perm_b32 v35, v235, v227, s50\n"
    "v_perm_b32 v36, v231, v239, s50\n"
    "v_perm_b32 v37, v79, v75, s51\n"
    "v_perm_b32 v38, v235, v227, s51\n"
    "v_perm_b32 v39, v231, v239, s51\n"
    "v_cvt_pk_bf16_f32 v46, v29, v32\n"
    "v_cvt_pk_bf16_f32 v50, v30, v33\n"
    "v_dot2_f32_bf16 v35, v34, v35, 0 neg_hi:[0,1,0]\n"
    "v_dot2_f32_bf16 v36, v34, v36, 0\n"
    "v_dot2_f32_bf16 v38, v37, v38, 0 neg_hi:[0,1,0]\n"
    "v_dot2_f32_bf16 v39, v37, v39, 0\n"
    "s_nop 1\n"
    "v_cvt_pk_bf16_f32 v47, v35, v38\n"
    "v_cvt_pk_bf16_f32 v51, v36, v39\n"
    "ds_write_b128 v5, v[44:47] offset:4096\n"
    "ds_write_b128 v6, v[48:51] offset:4096\n"
    "global_load_dwordx4 v[88:91], v10, s[40:41] offset:0\n"
    "global_load_dwordx4 v[92:95], v10, s[40:41] offset:128\n"
    "global_load_dwordx4 v[224:227], v10, s[54:55] offset:-2048\n"
    "global_load_dwordx4 v[228:231], v10, s[54:55] offset:-1920\n"
    "global_load_dwordx4 v[232:235], v10, s[56:57] offset:-2048\n"
    "global_load_dwordx4 v[236:239], v10, s[56:57] offset:-1920\n"
    "ds_read_b128 v[180:183], v134\n"
    "v_add_u32_e32 v134, 0x200, v134\n"
    "s_waitcnt vmcnt(19)\n"
    "v_cvt_f32_i32_e32 v172, v172\n"
    "v_cvt_f32_i32_e32 v173, v173\n"
    "v_cvt_f32_i32_e32 v174, v174\n"
    "v_cvt_f32_i32_e32 v175, v175\n"
    "v_cvt_f32_i32_e32 v176, v176\n"
    "v_cvt_f32_i32_e32 v177, v177\n"
    "v_cvt_f32_i32_e32 v178, v178\n"
    "v_cvt_f32_i32_e32 v179, v179\n"
    "s_waitcnt lgkmcnt(0)\n"
    "v_fma_f32 v172, v172, v180, v181\n"
    "v_fma_f32 v173, v173, v180, v181\n"
    "v_fma_f32 v174, v174, v180, v181\n"
    "v_fma_f32 v175, v175, v180, v181\n"
    "v_fma_f32 v176, v176, v182, v183\n"
    "v_fma_f32 v177, v177, v182, v183\n"
    "v_fma_f32 v178, v178, v182, v183\n"
    "v_fma_f32 v179, v179, v182, v183\n"
    "v_cvt_pk_bf16_f32 v184, v172, v173\n"
    "v_cvt_pk_bf16_f32 v185, v174, v175\n"
    "v_cvt_pk_bf16_f32 v186, v176, v177\n"
    "v_cvt_pk_bf16_f32 v187, v178, v179\n"
    "ds_write_b128 v135, v[184:187]\n"
    "ds_read_b64_tr_b16 v[188:189], v136\n"
    "ds_read_b64_tr_b16 v[190:191], v136 offset:512\n"
    "s_waitcnt lgkmcnt(0)\n"
    "buffer_store_dwordx4 v[188:191], v132, s[68:71], s58 offen sc1\n"
    "s_add_u32 s58, s58, 0x10000\n"
    "s_nop 0\n"
    "s_waitcnt vmcnt(14)\n"
    "v_perm_b32 v28, v108, v104, s50\n"
    "v_perm_b32 v29, v200, v192, s50\n"
    "v_perm_b32 v30, v196, v204, s50\n"
    "v_perm_b32 v31, v108, v104, s51\n"
    "v_perm_b32 v32, v200, v192, s51\n"
    "v_perm_b32 v33, v196, v204, s51\n"
    "v_dot2_f32_bf16 v29, v28, v29, 0 neg_hi:[0,1,0]\n"
    "v_dot2_f32_bf16 v30, v28, v30, 0\n"
    "v_dot2_f32_bf16 v32, v31, v32, 0 neg_hi:[0,1,0]\n"
    "v_dot2_f32_bf16 v33, v31, v33, 0\n"
    "v_perm_b32 v34, v109, v105, s50\n"
    "v_perm_b32 v35, v201, v193, s50\n"
    "v_perm_b32 v36, v197, v205, s50\n"
    "v_perm_b32 v37, v109, v105, s51\n"
    "v_perm_b32 v38, v201, v193, s51\n"
    "v_perm_b32 v39, v197, v205, s51\n"
    "v_cvt_pk_bf16_f32 v44, v29, v32\n"
    "v_cvt_pk_bf16_f32 v48, v30, v33\n"
    "v_dot2_f32_bf16 v35, v34, v35, 0 neg_hi:[0,1,0]\n"
    "v_dot2_f32_bf16 v36, v34, v36, 0\n"
    "v_dot2_f32_bf16 v38, v37, v38, 0 neg_hi:[0,1,0]\n"
    "v_dot2_f32_bf16 v39, v37, v39, 0\n"
    "v_perm_b32 v28, v110, v106, s50\n"
    "v_perm_b32 v29, v202, v194, s50\n"
    "v_perm_b32 v30, v198, v206, s50\n"
    "v_perm_b32 v31, v110, v106, s51\n"
    "v_perm_b32 v32, v202, v194, s51\n"
    "v_perm_b32 v33, v198, v206, s51\n"
    "v_cvt_pk_bf16_f32 v45, v35, v38\n"
    "v_cvt_pk_bf16_f32 v49, v36, v39\n"
    "v_dot2_f32_bf16 v29, v28, v29, 0 neg_hi:[0,1,0]\n"
    "v_dot2_f32_bf16 v30, v28, v30, 0\n"
    "v_dot2_f32_bf16 v32, v31, v32, 0 neg_hi:[0,1,0]\n"
    "v_dot2_f32_bf16 v33, v31, v33, 0\n"
    "v_perm_b32 v34, v111, v107, s50\n"
    "v_perm_b32 v35, v203, v195, s50\n"
    "v_perm_b32 v36, v199, v207, s50\n"
    "v_perm_b32 v37, v111, v107, s51\n"
    "v_perm_b32 v38, v203, v195, s51\n"
    "v_perm_b32 v39, v199, v207, s51\n"
    "v_cvt_pk_bf16_f32 v46, v29, v32\n"
    "v_cvt_pk_bf16_f32 v50, v30, v33\n"
    "v_dot2_f32_bf16 v35, v34, v35, 0 neg_hi:[0,1,0]\n"
    "v_dot2_f32_bf16 v36, v34, v36, 0\n"
    "v_dot2_f32_bf16 v38, v37, v38, 0 neg_hi:[0,1,0]\n"
    "v_dot2_f32_bf16 v39, v37, v39, 0\n"
    "s_nop 1\n"
    "v_cvt_pk_bf16_f32 v47, v35, v38\n"
    "v_cvt_pk_bf16_f32 v51, v36, v39\n"
    "ds_write_b128 v6, v[44:47] offset:6144\n"
    "ds_write_b128 v5, v[48:51] offset:6144\n"
    "ds_read_b128 a[144:147], v52 offset:4096\n"
    "ds_read_b128 a[148:151], v53 offset:4096\n"
    "ds_read_b128 a[152:155], v54 offset:4096\n"
    "ds_read_b128 a[156:159], v55 offset:4096\n"
    "global_load_dwordx4 v[120:123], v10, s[40:41] offset:2048\n"
    "global_load_dwordx4 v[124:127], v10, s[40:41] offset:2176\n"
    "global_load_dwordx4 v[192:195], v10, s[54:55] offset:0\n"
    "global_load_dwordx4 v[196:199], v10, s[54:55] offset:128\n"
    "global_load_dwordx4 v[200:203], v10, s[56:57] offset:0\n"
    "global_load_dwordx4 v[204:207], v10, s[56:57] offset:128\n"
    "s_waitcnt vmcnt(14)\n"
    "v_perm_b32 v28, v84, v80, s50\n"
    "v_perm_b32 v29, v216, v208, s50\n"
    "v_perm_b32 v30, v212, v220, s50\n"
    "v_perm_b32 v31, v84, v80, s51\n"
    "v_perm_b32 v32, v216, v208, s51\n"
    "v_perm_b32 v33, v212, v220, s51\n"
    "v_dot2_f32_bf16 v29, v28, v29, 0 neg_hi:[0,1,0]\n"
    "v_dot2_f32_bf16 v30, v28, v30, 0\n"
    "v_dot2_f32_bf16 v32, v31, v32, 0 neg_hi:[0,1,0]\n"
    "v_dot2_f32_bf16 v33, v31, v33, 0\n"
    "v_perm_b32 v34, v85, v81, s50\n"
    "v_perm_b32 v35, v217, v209, s50\n"
    "v_perm_b32 v36, v213, v221, s50\n"
    "v_perm_b32 v37, v85, v81, s51\n"
    "v_perm_b32 v38, v217, v209, s51\n"
    "v_perm_b32 v39, v213, v221, s51\n"
    "v_cvt_pk_bf16_f32 v44, v29, v32\n"
    "v_cvt_pk_bf16_f32 v48, v30, v33\n"
    "v_dot2_f32_bf16 v35, v34, v35, 0 neg_hi:[0,1,0]\n"
    "v_dot2_f32_bf16 v36, v34, v36, 0\n"
    "v_dot2_f32_bf16 v38, v37, v38, 0 neg_hi:[0,1,0]\n"
    "v_dot2_f32_bf16 v39, v37, v39, 0\n"
    "v_perm_b32 v28, v86, v82, s50\n"
    "v_perm_b32 v29, v218, v210, s50\n"
    "v_perm_b32 v30, v214, v222, s50\n"
    "v_perm_b32 v31, v86, v82, s51\n"
    "v_perm_b32 v32, v218, v210, s51\n"
    "v_perm_b32 v33, v214, v222, s51\n"
    "v_cvt_pk_bf16_f32 v45, v35, v38\n"
    "v_cvt_pk_bf16_f32 v49, v36, v39\n"
    "v_dot2_f32_bf16 v29, v28, v29, 0 neg_hi:[0,1,0]\n"
    "v_dot2_f32_bf16 v30, v28, v30, 0\n"
    "v_dot2_f32_bf16 v32, v31, v32, 0 neg_hi:[0,1,0]\n"
    "v_dot2_f32_bf16 v33, v31, v33, 0\n"
    "v_perm_b32 v34, v87, v83, s50\n"
    "v_perm_b32 v35, v219, v211, s50\n"
    "v_perm_b32 v36, v215, v223, s50\n"
    "v_perm_b32 v37, v87, v83, s51\n"
    "v_perm_b32 v38, v219, v211, s51\n"
    "v_perm_b32 v39, v215, v223, s51\n"
    "v_cvt_pk_bf16_f32 v46, v29, v32\n"
    "v_cvt_pk_bf16_f32 v50, v30, v33\n"
    "v_dot2_f32_bf16 v35, v34, v35, 0 neg_hi:[0,1,0]\n"
    "v_dot2_f32_bf16 v36, v34, v36, 0\n"
    "v_dot2_f32_bf16 v38, v37, v38, 0 neg_hi:[0,1,0]\n"
    "v_dot2_f32_bf16 v39, v37, v39, 0\n"
    "s_nop 1\n"
    "v_cvt_pk_bf16_f32 v47, v35, v38\n"
    "v_cvt_pk_bf16_f32 v51, v36, v39\n"
    "ds_write_b128 v5, v[44:47] offset:0\n"
    "ds_write_b128 v6, v[48:51] offset:0\n"
    "s_waitcnt vmcnt(13)\n"
    "s_cmp_lg_u32 s37, 0\n"
    "s_cbranch_scc1 .Lval_done\n"
    "s_mov_b64 exec, 0xff\n"
    "s_branch .Lval_chk\n"
    ".Lval_loop:\n"
    "buffer_load_dwordx2 v[56:57], v61, s[88:91], 0 offen sc1\n"
    "s_waitcnt vmcnt(0)\n"
    ".Lval_chk:\n"
    "v_cmp_ne_u32_e32 vcc, v57, v63\n"
    "v_cmp_lt_u32_e64 s[66:67], 61, v56\n"
    "s_or_b64 vcc, vcc, s[66:67]\n"
    "s_cbranch_vccz .Lval_ok\n"
    "s_sub_u32 s97, s97, 1\n"
    "s_cbranch_scc0 .Lval_loop\n"
    "v_mov_b32_e32 v61, 0x7fffff00\n"
    "v_mov_b32_e32 v56, 0x25000\n"
    "ds_write_b32 v56, v56\n"
    "s_waitcnt lgkmcnt(0)\n"
    "s_branch .Lval_end\n"
    ".Lval_ok:\n"
    "s_mov_b64 exec, 1\n"
    "v_mov_b32_e32 v56, 0x400\n"
    "buffer_store_dword v63, v56, s[88:91], 0 offen sc1\n"
    ".Lval_end:\n"
    "s_mov_b64 exec, -1\n"
    ".Lval_done:\n"
    "s_barrier\n"
    "s_mov_b32 s93, 0x0\n"
    "s_add_u32 m0, s92, 0x0\n"
    "s_nop 0\n"
    "buffer_load_dwordx4 v21, s[68:71], s93 offen offset:0 sc1 lds\n"
    "buffer_load_dwordx4 v21, s[68:71], s93 offen offset:1024 sc1 lds\n"
    "buffer_load_dwordx4 v21, s[68:71], s93 offen offset:2048 sc1 lds\n"
    "buffer_load_dwordx4 v21, s[68:71], s93 offen offset:3072 sc1 lds\n"
    "global_load_dwordx4 v[208:211], v10, s[54:55] offset:2048\n"
    "global_load_dwordx4 v[212:215], v10, s[54:55] offset:2176\n"
    "global_load_dwordx4 v[216:219], v10, s[56:57] offset:2048\n"
    "global_load_dwordx4 v[220:223], v10, s[56:57] offset:2176\n"
    "s_waitcnt vmcnt(15)\n"
    "v_perm_b32 v28, v116, v112, s50\n"
    "v_perm_b32 v29, v232, v224, s50\n"
    "v_perm_b32 v30, v228, v236, s50\n"
    "v_perm_b32 v31, v116, v112, s51\n"
    "v_perm_b32 v32, v232, v224, s51\n"
    "v_perm_b32 v33, v228, v236, s51\n"
    "v_dot2_f32_bf16 v29, v28, v29, 0 neg_hi:[0,1,0]\n"
    "v_dot2_f32_bf16 v30, v28, v30, 0\n"
    "v_dot2_f32_bf16 v32, v31, v32, 0 neg_hi:[0,1,0]\n"
    "v_dot2_f32_bf16 v33, v31, v33, 0\n"
    "v_perm_b32 v34, v117, v113, s50\n"
    "v_perm_b32 v35, v233, v225, s50\n"
    "v_perm_b32 v36, v229, v237, s50\n"
    "v_perm_b32 v37, v117, v113, s51\n"
    "v_perm_b32 v38, v233, v225, s51\n"
    "v_perm_b32 v39, v229, v237, s51\n"
    "v_cvt_pk_bf16_f32 v44, v29, v32\n"
    "v_cvt_pk_bf16_f32 v48, v30, v33\n"
    "v_dot2_f32_bf16 v35, v34, v35, 0 neg_hi:[0,1,0]\n"
    "v_dot2_f32_bf16 v36, v34, v36, 0\n"
    "v_dot2_f32_bf16 v38, v37, v38, 0 neg_hi:[0,1,0]\n"
    "v_dot2_f32_bf16 v39, v37, v39, 0\n"
    "v_perm_b32 v28, v118, v114, s50\n"
    "v_perm_b32 v29, v234, v226, s50\n"
    "v_perm_b32 v30, v230, v238, s50\n"
    "v_perm_b32 v31, v118, v114, s51\n"
    "v_perm_b32 v32, v234, v226, s51\n"
    "v_perm_b32 v33, v230, v238, s51\n"
    "v_cvt_pk_bf16_f32 v45, v35, v38\n"
    "v_cvt_pk_bf16_f32 v49, v36, v39\n"
    "v_dot2_f32_bf16 v29, v28, v29, 0 neg_hi:[0,1,0]\n"
    "v_dot2_f32_bf16 v30, v28, v30, 0\n"
    "v_dot2_f32_bf16 v32, v31, v32, 0 neg_hi:[0,1,0]\n"
    "v_dot2_f32_bf16 v33, v31, v33, 0\n"
    "v_perm_b32 v34, v119, v115, s50\n"
    "v_perm_b32 v35, v235, v227, s50\n"
    "v_perm_b32 v36, v231, v239, s50\n"
    "v_perm_b32 v37, v119, v115, s51\n"
    "v_perm_b32 v38, v235, v227, s51\n"
    "v_perm_b32 v39, v231, v239, s51\n"
    "v_cvt_pk_bf16_f32 v46, v29, v32\n"
    "v_cvt_pk_bf16_f32 v50, v30, v33\n"
    "v_dot2_f32_bf16 v35, v34, v35, 0 neg_hi:[0,1,0]\n"
    "v_dot2_f32_bf16 v36, v34, v36, 0\n"
    "v_dot2_f32_bf16 v38, v37, v38, 0 neg_hi:[0,1,0]\n"
    "v_dot2_f32_bf16 v39, v37, v39, 0\n"
    "s_nop 1\n"
    "v_cvt_pk_bf16_f32 v47, v35, v38\n"
    "v_cvt_pk_bf16_f32 v51, v36, v39\n"
    "ds_write_b128 v6, v[44:47] offset:2048\n"
    "ds_write_b128 v5, v[48:51] offset:2048\n"
    "ds_read_b128 a[160:163], v52 offset:0\n"
    "ds_read_b128 a[164:167], v53 offset:0\n"
    "ds_read_b128 a[168:171], v54 offset:0\n"
    "ds_read_b128 a[172:175], v55 offset:0\n"
    "buffer_load_dwordx4 v[140:143], v128, s[72:75], s42 offen\n"
    "buffer_load_dwordx4 v[144:147], v128, s[72:75], s42 offen offset:16\n"
    "s_add_u32 s42, s42, 0x2000\n"
    "s_min_u32 s42, s42, 0x1e000\n"
    "buffer_load_dwordx4 v[148:151], v129, s[80:83], s48 offen\n"
    "buffer_load_dwordx4 v[152:155], v129, s[84:87], s48 offen\n"
    "s_add_u32 s48, s48, 0x1000\n"
    "s_min_u32 s48, s48, 0xf000\n"
    "buffer_load_dwordx4 v[164:167], v130, s[76:79], s43 offen\n"
    "buffer_load_dwordx4 v[168:171], v130, s[76:79], s43 offen offset:128\n"
    "s_add_u32 s43, s43, 0x2000\n"
    "s_waitcnt vmcnt(14)\n"
    "v_perm_b32 v28, v92, v88, s50\n"
    "v_perm_b32 v29, v200, v192, s50\n"
    "v_perm_b32 v30, v196, v204, s50\n"
    "v_perm_b32 v31, v92, v88, s51\n"
    "v_perm_b32 v32, v200, v192, s51\n"
    "v_perm_b32 v33, v196, v204, s51\n"
    "v_dot2_f32_bf16 v29, v28, v29, 0 neg_hi:[0,1,0]\n"
    "v_dot2_f32_bf16 v30, v28, v30, 0\n"
    "v_dot2_f32_bf16 v32, v31, v32, 0 neg_hi:[0,1,0]\n"
    "v_dot2_f32_bf16 v33, v31, v33, 0\n"
    "v_perm_b32 v34, v93, v89, s50\n"
    "v_perm_b32 v35, v201, v193, s50\n"
    "v_perm_b32 v36, v197, v205, s50\n"
    "v_perm_b32 v37, v93, v89, s51\n"
    "v_perm_b32 v38, v201, v193, s51\n"
    "v_perm_b32 v39, v197, v205, s51\n"
    "v_cvt_pk_bf16_f32 v44, v29, v32\n"
    "v_cvt_pk_bf16_f32 v48, v30, v33\n"
    "v_dot2_f32_bf16 v35, v34, v35, 0 neg_hi:[0,1,0]\n"
    "v_dot2_f32_bf16 v36, v34, v36, 0\n"
    "v_dot2_f32_bf16 v38, v37, v38, 0 neg_hi:[0,1,0]\n"
    "v_dot2_f32_bf16 v39, v37, v39, 0\n"
    "v_perm_b32 v28, v94, v90, s50\n"
    "v_perm_b32 v29, v202, v194, s50\n"
    "v_perm_b32 v30, v198, v206, s50\n"
    "v_perm_b32 v31, v94, v90, s51\n"
    "v_perm_b32 v32, v202, v194, s51\n"
    "v_perm_b32 v33, v198, v206, s51\n"
    "v_cvt_pk_bf16_f32 v45, v35, v38\n"
    "v_cvt_pk_bf16_f32 v49, v36, v39\n"
    "v_dot2_f32_bf16 v29, v28, v29, 0 neg_hi:[0,1,0]\n"
    "v_dot2_f32_bf16 v30, v28, v30, 0\n"
    "v_dot2_f32_bf16 v32, v31, v32, 0 neg_hi:[0,1,0]\n"
    "v_dot2_f32_bf16 v33, v31, v33, 0\n"
    "v_perm_b32 v34, v95, v91, s50\n"
    "v_perm_b32 v35, v203, v195, s50\n"
    "v_perm_b32 v36, v199, v207, s50\n"
    "v_perm_b32 v37, v95, v91, s51\n"
    "v_perm_b32 v38, v203, v195, s51\n"
    "v_perm_b32 v39, v199, v207, s51\n"
    "v_cvt_pk_bf16_f32 v46, v29, v32\n"
    "v_cvt_pk_bf16_f32 v50, v30, v33\n"
    "v_dot2_f32_bf16 v35, v34, v35, 0 neg_hi:[0,1,0]\n"
    "v_dot2_f32_bf16 v36, v34, v36, 0\n"
    "v_dot2_f32_bf16 v38, v37, v38, 0 neg_hi:[0,1,0]\n"
    "v_dot2_f32_bf16 v39, v37, v39, 0\n"
    "s_nop 1\n"
    "v_cvt_pk_bf16_f32 v47, v35, v38\n"
    "v_cvt_pk_bf16_f32 v51, v36, v39\n"
    "ds_write_b128 v5, v[44:47] offset:4096\n"
    "ds_write_b128 v6, v[48:51] offset:4096\n"
    "s_waitcnt vmcnt(6)\n"
    "v_perm_b32 v28, v124, v120, s50\n"
    "v_perm_b32 v29, v216, v208, s50\n"
    "v_perm_b32 v30, v212, v220, s50\n"
    "v_perm_b32 v31, v124, v120, s51\n"
    "v_perm_b32 v32, v216, v208, s51\n"
    "v_perm_b32 v33, v212, v220, s51\n"
    "v_dot2_f32_bf16 v29, v28, v29, 0 neg_hi:[0,1,0]\n"
    "v_dot2_f32_bf16 v30, v28, v30, 0\n"
    "v_dot2_f32_bf16 v32, v31, v32, 0 neg_hi:[0,1,0]\n"
    "v_dot2_f32_bf16 v33, v31, v33, 0\n"
    "v_perm_b32 v34, v125, v121, s50\n"
    "v_perm_b32 v35, v217, v209, s50\n"
    "v_perm_b32 v36, v213, v221, s50\n"
    "v_perm_b32 v37, v125, v121, s51\n"
    "v_perm_b32 v38, v217, v209, s51\n"
    "v_perm_b32 v39, v213, v221, s51\n"
    "v_cvt_pk_bf16_f32 v44, v29, v32\n"
    "v_cvt_pk_bf16_f32 v48, v30, v33\n"
    "v_dot2_f32_bf16 v35, v34, v35, 0 neg_hi:[0,1,0]\n"
    "v_dot2_f32_bf16 v36, v34, v36, 0\n"
    "v_dot2_f32_bf16 v38, v37, v38, 0 neg_hi:[0,1,0]\n"
    "v_dot2_f32_bf16 v39, v37, v39, 0\n"
    "v_perm_b32 v28, v126, v122, s50\n"
    "v_perm_b32 v29, v218, v210, s50\n"
    "v_perm_b32 v30, v214, v222, s50\n"
    "v_perm_b32 v31, v126, v122, s51\n"
    "v_perm_b32 v32, v218, v210, s51\n"
    "v_perm_b32 v33, v214, v222, s51\n"
    "v_cvt_pk_bf16_f32 v45, v35, v38\n"
    "v_cvt_pk_bf16_f32 v49, v36, v39\n"
    "v_dot2_f32_bf16 v29, v28, v29, 0 neg_hi:[0,1,0]\n"
    "v_dot2_f32_bf16 v30, v28, v30, 0\n"
    "v_dot2_f32_bf16 v32, v31, v32, 0 neg_hi:[0,1,0]\n"
    "v_dot2_f32_bf16 v33, v31, v33, 0\n"
    "v_perm_b32 v34, v127, v123, s50\n"
    "v_perm_b32 v35, v219, v211, s50\n"
    "v_perm_b32 v36, v215, v223, s50\n"
    "v_perm_b32 v37, v127, v123, s51\n"
    "v_perm_b32 v38, v219, v211, s51\n"
    "v_perm_b32 v39, v215, v223, s51\n"
    "v_cvt_pk_bf16_f32 v46, v29, v32\n"
    "v_cvt_pk_bf16_f32 v50, v30, v33\n"
    "v_dot2_f32_bf16 v35, v34, v35, 0 neg_hi:[0,1,0]\n"
    "v_dot2_f32_bf16 v36, v34, v36, 0\n"
    "v_dot2_f32_bf16 v38, v37, v38, 0 neg_hi:[0,1,0]\n"
    "v_dot2_f32_bf16 v39, v37, v39, 0\n"
    "s_nop 1\n"
    "v_cvt_pk_bf16_f32 v47, v35, v38\n"
    "v_cvt_pk_bf16_f32 v51, v36, v39\n"
    "ds_write_b128 v6, v[44:47] offset:6144\n"
    "ds_write_b128 v5, v[48:51] offset:6144\n"
    "ds_read_b128 a[176:179], v52 offset:4096\n"
    "ds_read_b128 a[180:183], v53 offset:4096\n"
    "ds_read_b128 a[184:187], v54 offset:4096\n"
    "ds_read_b128 a[188:191], v55 offset:4096\n"
    "v_mov_b32_e32 v232, 0\n"
    "v_mov_b32_e32 v233, 0\n"
    "v_mov_b32_e32 v234, 0\n"
    "v_mov_b32_e32 v235, 0\n"
    "v_mov_b32_e32 v236, 0\n"
    "v_mov_b32_e32 v237, 0\n"
    "v_mov_b32_e32 v238, 0\n"
    "v_mov_b32_e32 v239, 0\n"
    "v_xor_b32_e32 v200, 0x0, v20\n"
    "v_xor_b32_e32 v201, 0x10, v20\n"
    "v_xor_b32_e32 v202, 0x20, v20\n"
    "v_xor_b32_e32 v203, 0x30, v20\n"
    "v_xor_b32_e32 v204, 0x40, v20\n"
    "v_xor_b32_e32 v205, 0x50, v20\n"
    "v_xor_b32_e32 v206, 0x60, v20\n"
    "v_xor_b32_e32 v207, 0x70, v20\n"
    "v_bfe_u32 v7, v4, 1, 3\n"
    "v_lshrrev_b32_e32 v8, 5, v4\n"
    "v_cmp_eq_u32_e32 vcc, v7, v8\n"
    "v_and_b32_e32 v7, 1, v4\n"
    "v_mul_u32_u24_e32 v7, 0xaaaa, v7\n"
    "v_cndmask_b32_e32 v224, 0, v24, vcc\n"
    "v_cndmask_b32_e32 v225, 0, v24, vcc\n"
    "v_cndmask_b32_e32 v226, 0, v24, vcc\n"
    "v_cndmask_b32_e32 v227, 0, v24, vcc\n"
    "v_add_u32_e32 v228, 0x4444, v7\n"
    "s_lshl_b32 s66, s37, 8\n"
    "s_add_u32 s66, s66, 0x25400\n"
    "v_lshl_add_u32 v229, v4, 2, s66\n"
    "v_and_b32_e32 v7, 15, v4\n"
    "v_lshrrev_b32_e32 v8, 4, v4\n"
    "v_lshlrev_b32_e32 v7, 2, v7\n"
    "v_lshl_add_u32 v7, v8, 8, v7\n"
    "v_add_u32_e32 v230, 0x25400, v7\n"
    "v_mov_b32_e32 v7, -1\n"
    "ds_write_b32 v229, v7\n"
    "v_mov_b32_e32 v62, 60\n"
    "s_mov_b32 s60, 59\n"
    "s_waitcnt vmcnt(10) lgkmcnt(0)\n"
    "s_barrier\n"
    "ds_read_b128 a[192:195], v16 offset:0\n"
    "ds_read_b128 a[196:199], v17 offset:0\n"
    "ds_read_b128 a[200:203], v18 offset:0\n"
    "ds_read_b128 a[204:207], v19 offset:0\n"
    "ds_read_b128 a[208:211], v16 offset:4096\n"
    "ds_read_b128 a[212:215], v17 offset:4096\n"
    "ds_read_b128 a[216:219], v18 offset:4096\n"
    "ds_read_b128 a[220:223], v19 offset:4096\n"
    "ds_read_b64 v[138:139], v133\n"
    "v_add_u32_e32 v133, 0x200, v133\n"
    "ds_read_b128 v[180:183], v134\n"
    "v_add_u32_e32 v134, 0x200, v134\n"
    "buffer_store_dword v62, v60, s[88:91], 0 offen sc1\n"
    "s_mov_b32 s93, 0x0\n"
    "s_add_u32 m0, s92, 0x10000\n"
    "s_nop 0\n"
    "buffer_load_dwordx4 v22, s[68:71], s93 offen offset:0 sc1 lds\n"
    "buffer_load_dwordx4 v22, s[68:71], s93 offen offset:1024 sc1 lds\n"
    "buffer_load_dwordx4 v22, s[68:71], s93 offen offset:2048 sc1 lds\n"
    "buffer_load_dwordx4 v22, s[68:71], s93 offen offset:3072 sc1 lds\n"
    "s_mov_b32 s93, 0x8000\n"
    "s_add_u32 m0, s92, 0x4000\n"
    "s_nop 0\n"
    "buffer_load_dwordx4 v21, s[68:71], s93 offen offset:0 sc1 lds\n"
    "buffer_load_dwordx4 v21, s[68:71], s93 offen offset:1024 sc1 lds\n"
    "buffer_load_dwordx4 v21, s[68:71], s93 offen offset:2048 sc1 lds\n"
    "buffer_load_dwordx4 v21, s[68:71], s93 offen offset:3072 sc1 lds\n"
    "buffer_load_dwordx4 v[240:243], v128, s[72:75], s42 offen\n"
    "buffer_load_dwordx4 v[244:247], v128, s[72:75], s42 offen offset:16\n"
    "s_add_u32 s42, s42, 0x2000\n"
    "s_min_u32 s42, s42, 0x1e000\n"
    "buffer_load_dwordx4 v[248:251], v129, s[80:83], s48 offen\n"
    "buffer_load_dwordx4 v[252:255], v129, s[84:87], s48 offen\n"
    "s_add_u32 s48, s48, 0x1000\n"
    "s_min_u32 s48, s48, 0xf000\n"
    "v_add_u32_e32 v21, 0x10000, v21\n"
    "v_add_u32_e32 v22, 0x8000, v22\n"
    "s_mov_b32 s93, 0\n"
    "s_waitcnt lgkmcnt(0)\n"
    "v_mfma_f32_16x16x32_bf16 v[64:67], a[192:195], a[128:131], 0\n"
    "s_waitcnt vmcnt(15)\n"
    "v_cvt_f32_i32_e32 v140, v140\n"
    "v_cvt_f32_i32_e32 v141, v141\n"
    "v_mfma_f32_16x16x32_bf16 v[64:67], a[196:199], a[132:135], v[64:67]\n"
    "v_cvt_f32_i32_e32 v142, v142\n"
    "v_cvt_f32_i32_e32 v143, v143\n"
    "v_cvt_f32_i32_e32 v144, v144\n"
    "v_mfma_f32_16x16x32_bf16 v[64:67], a[200:203], a[136:139], v[64:67]\n"
    "v_cvt_f32_i32_e32 v145, v145\n"
    "v_cvt_f32_i32_e32 v146, v146\n"
    "v_cvt_f32_i32_e32 v147, v147\n"
    "v_mfma_f32_16x16x32_bf16 v[64:67], a[204:207], a[140:143], v[64:67]\n"
    "v_fma_f32 v140, v140, v138, v139\n"
    "v_fma_f32 v141, v141, v138, v139\n"
    "v_fma_f32 v142, v142, v138, v139\n"
    "v_mfma_f32_16x16x32_bf16 v[68:71], a[208:211], a[128:131], 0\n"
    "v_fma_f32 v143, v143, v138, v139\n"
    "v_fma_f32 v144, v144, v138, v139\n"
    "v_fma_f32 v145, v145, v138, v139\n"
    "v_mfma_f32_16x16x32_bf16 v[68:71], a[212:215], a[132:135], v[68:71]\n"
    "v_fma_f32 v146, v146, v138, v139\n"
    "v_fma_f32 v147, v147, v138, v139\n"
    "v_lshlrev_b32_e32 v156, 16, v148\n"
    "v_mfma_f32_16x16x32_bf16 v[68:71], a[216:219], a[136:139], v[68:71]\n"
    "v_and_b32_e32 v157, s95, v148\n"
    "v_lshl_add_u32 v158, v152, 16, v137\n"
    "v_bitop3_b32 v159, v152, s95, v137 bitop3:0x6a\n"
    "v_mfma_f32_16x16x32_bf16 v[68:71], a[220:223], a[140:143], v[68:71]\n"
    "v_mul_f32_e32 v156, v140, v156\n"
    "v_mul_f32_e32 v157, v141, v157\n"
    "v_fmac_f32_dpp v156, v140, v158 quad_perm:[1,0,3,2] row_mask:0xf bank_mask:0xf\n"
    "v_mfma_f32_16x16x32_bf16 v[72:75], a[192:195], a[144:147], 0\n"
    "v_fmac_f32_dpp v157, v141, v159 quad_perm:[1,0,3,2] row_mask:0xf bank_mask:0xf\n"
    "v_cvt_pk_bf16_f32 v160, v156, v157\n"
    "v_lshlrev_b32_e32 v156, 16, v149\n"
    "v_mfma_f32_16x16x32_bf16 v[72:75], a[196:199], a[148:151], v[72:75]\n"
    "v_and_b32_e32 v157, s95, v149\n"
    "v_lshl_add_u32 v158, v153, 16, v137\n"
    "v_bitop3_b32 v159, v153, s95, v137 bitop3:0x6a\n"
    "v_mfma_f32_16x16x32_bf16 v[72:75], a[200:203], a[152:155], v[72:75]\n"
    "v_mul_f32_e32 v156, v142, v156\n"
    "v_mul_f32_e32 v157, v143, v157\n"
    "v_fmac_f32_dpp v156, v142, v158 quad_perm:[1,0,3,2] row_mask:0xf bank_mask:0xf\n"
    "v_mfma_f32_16x16x32_bf16 v[72:75], a[204:207], a[156:159], v[72:75]\n"
    "v_fmac_f32_dpp v157, v143, v159 quad_perm:[1,0,3,2] row_mask:0xf bank_mask:0xf\n"
    "v_cvt_pk_bf16_f32 v161, v156, v157\n"
    "v_lshlrev_b32_e32 v156, 16, v150\n"
    "v_mfma_f32_16x16x32_bf16 v[76:79], a[208:211], a[144:147], 0\n"
    "v_and_b32_e32 v157, s95, v150\n"
    "v_lshl_add_u32 v158, v154, 16, v137\n"
    "v_bitop3_b32 v159, v154, s95, v137 bitop3:0x6a\n"
    "v_mfma_f32_16x16x32_bf16 v[76:79], a[212:215], a[148:151], v[76:79]\n"
    "v_mul_f32_e32 v156, v144, v156\n"
    "v_mul_f32_e32 v157, v145, v157\n"
    "v_fmac_f32_dpp v156, v144, v158 quad_perm:[1,0,3,2] row_mask:0xf bank_mask:0xf\n"
    "v_mfma_f32_16x16x32_bf16 v[76:79], a[216:219], a[152:155], v[76:79]\n"
    "v_fmac_f32_dpp v157, v145, v159 quad_perm:[1,0,3,2] row_mask:0xf bank_mask:0xf\n"
    "v_cvt_pk_bf16_f32 v162, v156, v157\n"
    "v_lshlrev_b32_e32 v156, 16, v151\n"
    "v_mfma_f32_16x16x32_bf16 v[76:79], a[220:223], a[156:159], v[76:79]\n"
    "v_and_b32_e32 v157, s95, v151\n"
    "v_lshl_add_u32 v158, v155, 16, v137\n"
    "v_bitop3_b32 v159, v155, s95, v137 bitop3:0x6a\n"
    "v_mfma_f32_16x16x32_bf16 v[80:83], a[192:195], a[160:163], 0\n"
    "v_mul_f32_e32 v156, v146, v156\n"
    "v_mul_f32_e32 v157, v147, v157\n"
    "v_fmac_f32_dpp v156, v146, v158 quad_perm:[1,0,3,2] row_mask:0xf bank_mask:0xf\n"
    "v_mfma_f32_16x16x32_bf16 v[80:83], a[196:199], a[164:167], v[80:83]\n"
    "v_fmac_f32_dpp v157, v147, v159 quad_perm:[1,0,3,2] row_mask:0xf bank_mask:0xf\n"
    "v_cvt_pk_bf16_f32 v163, v156, v157\n"
    "buffer_store_dwordx4 v[160:163], v131, s[68:71], s49 offen sc1\n"
    "v_mfma_f32_16x16x32_bf16 v[80:83], a[200:203], a[168:171], v[80:83]\n"
    "s_add_u32 s49, s49, 0x10000\n"
    "ds_read_b64 v[138:139], v133\n"
    "v_add_u32_e32 v133, 0x200, v133\n"
    "v_mfma_f32_16x16x32_bf16 v[80:83], a[204:207], a[172:175], v[80:83]\n"
    "v_max3_f32 v5, v64, v65, v66\n"
    "v_max3_f32 v6, v67, v68, v69\n"
    "v_max3_f32 v5, v5, v70, v71\n"
    "v_mfma_f32_16x16x32_bf16 v[84:87], a[208:211], a[160:163], 0\n"
    "v_max_f32_e32 v5, v5, v6\n"
    "v_mov_b32_e32 v6, v5\n"
    "s_nop 1\n"
    "v_mfma_f32_16x16x32_bf16 v[84:87], a[212:215], a[164:167], v[84:87]\n"
    "v_permlane32_swap_b32_e32 v5, v6\n"
    "v_max_f32_e32 v5, v5, v6\n"
    "v_mov_b32_e32 v6, v5\n"
    "v_mfma_f32_16x16x32_bf16 v[84:87], a[216:219], a[168:171], v[84:87]\n"
    "s_nop 1\n"
    "v_permlane16_swap_b32_e32 v5, v6\n"
    "v_max_f32_e32 v5, v5, v6\n"
    "v_mfma_f32_16x16x32_bf16 v[84:87], a[220:223], a[172:175], v[84:87]\n"
    "v_sub_f32_e32 v64, v64, v5\n"
    "v_sub_f32_e32 v65, v65, v5\n"
    "v_sub_f32_e32 v66, v66, v5\n"
    "v_mfma_f32_16x16x32_bf16 v[88:91], a[192:195], a[176:179], 0\n"
    "v_sub_f32_e32 v67, v67, v5\n"
    "v_sub_f32_e32 v68, v68, v5\n"
    "v_sub_f32_e32 v69, v69, v5\n"
    "v_mfma_f32_16x16x32_bf16 v[88:91], a[196:199], a[180:183], v[88:91]\n"
    "v_sub_f32_e32 v70, v70, v5\n"
    "v_sub_f32_e32 v71, v71, v5\n"
    "v_sub_f32_e32 v44, 0, v5\n"
    "v_mfma_f32_16x16x32_bf16 v[88:91], a[200:203], a[184:187], v[88:91]\n"
    "v_sub_f32_e32 v45, 0, v5\n"
    "v_sub_f32_e32 v46, 0, v5\n"
    "v_sub_f32_e32 v47, 0, v5\n"
    "v_mfma_f32_16x16x32_bf16 v[88:91], a[204:207], a[188:191], v[88:91]\n"
    "v_exp_f32_e32 v71, v71\n"
    "v_exp_f32_e32 v70, v70\n"
    "v_exp_f32_e32 v69, v69\n"
    "v_mfma_f32_16x16x32_bf16 v[92:95], a[208:211], a[176:179], 0\n"
    "v_exp_f32_e32 v68, v68\n"
    "v_exp_f32_e32 v67, v67\n"
    "v_exp_f32_e32 v66, v66\n"
    "v_mfma_f32_16x16x32_bf16 v[92:95], a[212:215], a[180:183], v[92:95]\n"
    "v_exp_f32_e32 v65, v65\n"
    "v_exp_f32_e32 v64, v64\n"
    "s_nop 0\n"
    "v_mfma_f32_16x16x32_bf16 v[92:95], a[216:219], a[184:187], v[92:95]\n"
    "v_cvt_pk_bf16_f32 v71, v70, v71\n"
    "v_cvt_pk_bf16_f32 v70, v68, v69\n"
    "v_cvt_pk_bf16_f32 v69, v66, v67\n"
    "v_mfma_f32_16x16x32_bf16 v[92:95], a[220:223], a[188:191], v[92:95]\n"
    "v_cvt_pk_bf16_f32 v68, v64, v65\n"
    "v_max3_f32 v5, v72, v73, v74\n"
    "v_max3_f32 v6, v75, v76, v77\n"
    "ds_read_b128 a[192:195], v16 offset:8192\n"
    "ds_read_b128 a[196:199], v17 offset:8192\n"
    "ds_read_b128 a[200:203], v18 offset:8192\n"
    "ds_read_b128 a[204:207], v19 offset:8192\n"
    "ds_read_b128 a[208:211], v16 offset:12288\n"
    "ds_read_b128 a[212:215], v17 offset:12288\n"
    "ds_read_b128 a[216:219], v18 offset:12288\n"
    "ds_read_b128 a[220:223], v19 offset:12288\n"
    "v_max3_f32 v5, v5, v78, v79\n"
    "v_max_f32_e32 v5, v5, v6\n"
    "v_mov_b32_e32 v6, v5\n"
    "s_nop 1\n"
    "v_permlane32_swap_b32_e32 v5, v6\n"
    "v_max_f32_e32 v5, v5, v6\n"
    "v_mov_b32_e32 v6, v5\n"
    "s_nop 1\n"
    "v_permlane16_swap_b32_e32 v5, v6\n"
    "v_max_f32_e32 v5, v5, v6\n"
    "v_sub_f32_e32 v72, v72, v5\n"
    "v_sub_f32_e32 v73, v73, v5\n"
    "v_sub_f32_e32 v74, v74, v5\n"
    "v_sub_f32_e32 v75, v75, v5\n"
    "v_sub_f32_e32 v76, v76, v5\n"
    "v_sub_f32_e32 v77, v77, v5\n"
    "v_sub_f32_e32 v78, v78, v5\n"
    "v_sub_f32_e32 v79, v79, v5\n"
    "v_sub_f32_e32 v48, 0, v5\n"
    "v_sub_f32_e32 v49, 0, v5\n"
    "v_sub_f32_e32 v50, 0, v5\n"
    "v_sub_f32_e32 v51, 0, v5\n"
    "v_exp_f32_e32 v72, v72\n"
    "v_exp_f32_e32 v73, v73\n"
    "v_exp_f32_e32 v74, v74\n"
    "v_exp_f32_e32 v75, v75\n"
    "v_exp_f32_e32 v76, v76\n"
    "v_exp_f32_e32 v77, v77\n"
    "v_exp_f32_e32 v78, v78\n"
    "v_exp_f32_e32 v79, v79\n"
    "s_nop 0\n"
    "v_cvt_pk_bf16_f32 v72, v72, v73\n"
    "v_cvt_pk_bf16_f32 v73, v74, v75\n"
    "v_cvt_pk_bf16_f32 v74, v76, v77\n"
    "v_cvt_pk_bf16_f32 v75, v78, v79\n"
    "v_max3_f32 v5, v80, v81, v82\n"
    "v_max3_f32 v6, v83, v84, v85\n"
    "v_max3_f32 v5, v5, v86, v87\n"
    "v_max_f32_e32 v5, v5, v6\n"
    "v_mov_b32_e32 v6, v5\n"
    "s_nop 1\n"
    "v_permlane32_swap_b32_e32 v5, v6\n"
    "v_max_f32_e32 v5, v5, v6\n"
    "v_mov_b32_e32 v6, v5\n"
    "s_nop 1\n"
    "v_permlane16_swap_b32_e32 v5, v6\n"
    "v_max_f32_e32 v5, v5, v6\n"
    "v_sub_f32_e32 v80, v80, v5\n"
    "v_sub_f32_e32 v81, v81, v5\n"
    "v_sub_f32_e32 v82, v82, v5\n"
    "v_sub_f32_e32 v83, v83, v5\n"
    "v_sub_f32_e32 v84, v84, v5\n"
    "v_sub_f32_e32 v85, v85, v5\n"
    "v_sub_f32_e32 v86, v86, v5\n"
    "v_sub_f32_e32 v87, v87, v5\n"
    "v_sub_f32_e32 v52, 0, v5\n"
    "v_sub_f32_e32 v53, 0, v5\n"
    "v_sub_f32_e32 v54, 0, v5\n"
    "v_sub_f32_e32 v55, 0, v5\n"
    "v_exp_f32_e32 v87, v87\n"
    "v_exp_f32_e32 v86, v86\n"
    "v_exp_f32_e32 v85, v85\n"
    "v_exp_f32_e32 v84, v84\n"
    "v_exp_f32_e32 v83, v83\n"
    "v_exp_f32_e32 v82, v82\n"
    "v_exp_f32_e32 v81, v81\n"
    "v_exp_f32_e32 v80, v80\n"
    "s_nop 0\n"
    "v_cvt_pk_bf16_f32 v87, v86, v87\n"
    "v_cvt_pk_bf16_f32 v86, v84, v85\n"
    "v_cvt_pk_bf16_f32 v85, v82, v83\n"
    "v_cvt_pk_bf16_f32 v84, v80, v81\n"
    "s_waitcnt vmcnt(14)\n"
    "v_cvt_f32_i32_e32 v164, v164\n"
    "v_cvt_f32_i32_e32 v165, v165\n"
    "v_cvt_f32_i32_e32 v166, v166\n"
    "v_cvt_f32_i32_e32 v167, v167\n"
    "v_cvt_f32_i32_e32 v168, v168\n"
    "v_cvt_f32_i32_e32 v169, v169\n"
    "v_cvt_f32_i32_e32 v170, v170\n"
    "v_cvt_f32_i32_e32 v171, v171\n"
    "v_fma_f32 v164, v164, v180, v181\n"
    "v_fma_f32 v165, v165, v180, v181\n"
    "v_fma_f32 v166, v166, v180, v181\n"
    "v_fma_f32 v167, v167, v180, v181\n"
    "v_fma_f32 v168, v168, v182, v183\n"
    "v_fma_f32 v169, v169, v182, v183\n"
    "v_fma_f32 v170, v170, v182, v183\n"
    "v_fma_f32 v171, v171, v182, v183\n"
    "v_cvt_pk_bf16_f32 v184, v164, v165\n"
    "v_cvt_pk_bf16_f32 v185, v166, v167\n"
    "v_cvt_pk_bf16_f32 v186, v168, v169\n"
    "v_cvt_pk_bf16_f32 v187, v170, v171\n"
    "ds_write_b128 v135, v[184:187]\n"
    "ds_read_b64_tr_b16 v[188:189], v136\n"
    "ds_read_b64_tr_b16 v[190:191], v136 offset:512\n"
    "v_max3_f32 v5, v88, v89, v90\n"
    "v_max3_f32 v6, v91, v92, v93\n"
    "v_max3_f32 v5, v5, v94, v95\n"
    "v_max_f32_e32 v5, v5, v6\n"
    "v_mov_b32_e32 v6, v5\n"
    "s_nop 1\n"
    "v_permlane32_swap_b32_e32 v5, v6\n"
    "v_max_f32_e32 v5, v5, v6\n"
    "v_mov_b32_e32 v6, v5\n"
    "s_nop 1\n"
    "v_permlane16_swap_b32_e32 v5, v6\n"
    "v_max_f32_e32 v5, v5, v6\n"
    "v_sub_f32_e32 v88, v88, v5\n"
    "v_sub_f32_e32 v89, v89, v5\n"
    "v_sub_f32_e32 v90, v90, v5\n"
    "v_sub_f32_e32 v91, v91, v5\n"
    "v_sub_f32_e32 v92, v92, v5\n"
    "v_sub_f32_e32 v93, v93, v5\n"
    "v_sub_f32_e32 v94, v94, v5\n"
    "v_sub_f32_e32 v95, v95, v5\n"
    "v_sub_f32_e32 v56, 0, v5\n"
    "v_sub_f32_e32 v57, 0, v5\n"
    "v_sub_f32_e32 v58, 0, v5\n"
    "v_sub_f32_e32 v59, 0, v5\n"
    "v_exp_f32_e32 v88, v88\n"
    "v_exp_f32_e32 v89, v89\n"
    "s_nop 0\n"
    "v_cvt_pk_bf16_f32 v88, v88, v89\n"
    "s_waitcnt vmcnt(1) lgkmcnt(0)\n"
    "v_cvt_f32_i32_e32 v240, v240\n"
    "v_cvt_f32_i32_e32 v241, v241\n"
    "v_cvt_f32_i32_e32 v242, v242\n"
    "v_cvt_f32_i32_e32 v243, v243\n"
    "v_cvt_f32_i32_e32 v244, v244\n"
    "v_cvt_f32_i32_e32 v245, v245\n"
    "v_cvt_f32_i32_e32 v246, v246\n"
    "v_cvt_f32_i32_e32 v247, v247\n"
    "v_fma_f32 v240, v240, v138, v139\n"
    "v_fma_f32 v241, v241, v138, v139\n"
    "v_fma_f32 v242, v242, v138, v139\n"
    "v_fma_f32 v243, v243, v138, v139\n"
    "v_mov_b32_e32 v62, 59\n"
    "buffer_load_dwordx4 v[172:175], v130, s[76:79], s43 offen\n"
    "buffer_load_dwordx4 v[176:179], v130, s[76:79], s43 offen offset:128\n"
    "s_add_u32 s43, s43, 0x2000\n"
    "buffer_load_dwordx4 v[140:143], v128, s[72:75], s42 offen\n"
    "buffer_load_dwordx4 v[144:147], v128, s[72:75], s42 offen offset:16\n"
    "s_add_u32 s42, s42, 0x2000\n"
    "s_min_u32 s42, s42, 0x1e000\n"
    "buffer_load_dwordx4 v[148:151], v129, s[80:83], s48 offen\n"
    "buffer_load_dwordx4 v[152:155], v129, s[84:87], s48 offen\n"
    "s_add_u32 s48, s48, 0x1000\n"
    "s_min_u32 s48, s48, 0xf000\n"
    "s_waitcnt vmcnt(6)\n"
    "s_mov_b32 s96, 61\n"
    "s_waitcnt lgkmcnt(0)\n"
    "s_mov_b32 s94, 5\n"
    "s_barrier\n"
    ".p2align 6\n"
    ".Lattn_loop:\n"
    "v_mfma_f32_16x16x32_bf16 v[96:99], a[192:195], a[128:131], v[44:47]\n"
    "v_exp_f32_e32 v90, v90\n"
    "v_mfma_f32_16x16x32_bf16 v[96:99], a[196:199], a[132:135], v[96:99]\n"
    "buffer_store_dword v62, v60, s[88:91], 0 offen sc1\n"
    "v_mfma_f32_16x16x32_bf16 v[96:99], a[200:203], a[136:139], v[96:99]\n"
    "s_add_u32 m0, s92, 0x14000\n"
    "v_exp_f32_e32 v91, v91\n"
    "v_mfma_f32_16x16x32_bf16 v[96:99], a[204:207], a[140:143], v[96:99]\n"
    "s_sub_u32 s96, s96, 1\n"
    "ds_read_b128 a[224:227], v200 offset:0\n"
    "v_cvt_pk_bf16_f32 v89, v90, v91\n"
    "v_mfma_f32_16x16x32_bf16 v[100:103], a[208:211], a[128:131], v[44:47]\n"
    "s_sub_u32 s60, s60, 1\n"
    "v_exp_f32_e32 v92, v92\n"
    "v_mfma_f32_16x16x32_bf16 v[100:103], a[212:215], a[132:135], v[100:103]\n"
    "buffer_load_dwordx4 v22, s[68:71], s93 offen offset:0 sc1 lds\n"
    "v_mfma_f32_16x16x32_bf16 v[100:103], a[216:219], a[136:139], v[100:103]\n"
    "v_exp_f32_e32 v93, v93\n"
    "v_mfma_f32_16x16x32_bf16 v[100:103], a[220:223], a[140:143], v[100:103]\n"
    "ds_read_b128 a[228:231], v201 offset:0\n"
    "v_cvt_pk_bf16_f32 v90, v92, v93\n"
    "v_mfma_f32_16x16x32_bf16 v[104:107], a[192:195], a[144:147], v[48:51]\n"
    "v_exp_f32_e32 v94, v94\n"
    "v_mfma_f32_16x16x32_bf16 v[104:107], a[196:199], a[148:151], v[104:107]\n"
    "buffer_load_dwordx4 v22, s[68:71], s93 offen offset:1024 sc1 lds\n"
    "v_mfma_f32_16x16x32_bf16 v[104:107], a[200:203], a[152:155], v[104:107]\n"
    "v_exp_f32_e32 v95, v95\n"
    "v_mfma_f32_16x16x32_bf16 v[104:107], a[204:207], a[156:159], v[104:107]\n"
    "ds_read_b128 a[232:235], v202 offset:0\n"
    "v_cvt_pk_bf16_f32 v91, v94, v95\n"
    "v_mfma_f32_16x16x32_bf16 v[108:111], a[208:211], a[144:147], v[48:51]\n"
    "v_exp_f32_e32 v103, v103\n"
    "v_mfma_f32_16x16x32_bf16 v[108:111], a[212:215], a[148:151], v[108:111]\n"
    "ds_read_b128 a[236:239], v203 offset:0\n"
    "v_fma_f32 v244, v244, v138, v139\n"
    "v_mfma_f32_16x16x32_bf16 v[108:111], a[216:219], a[152:155], v[108:111]\n"
    "v_exp_f32_e32 v102, v102\n"
    "v_mfma_f32_16x16x32_bf16 v[108:111], a[220:223], a[156:159], v[108:111]\n"
    "ds_read_b128 a[240:243], v204 offset:0\n"
    "v_cvt_pk_bf16_f32 v103, v102, v103\n"
    "v_mfma_f32_16x16x32_bf16 v[112:115], a[192:195], a[160:163], v[52:55]\n"
    "buffer_load_dwordx4 v22, s[68:71], s93 offen offset:2048 sc1 lds\n"
    "v_mfma_f32_16x16x32_bf16 v[112:115], a[196:199], a[164:167], v[112:115]\n"
    "v_exp_f32_e32 v101, v101\n"
    "v_mfma_f32_16x16x32_bf16 v[112:115], a[200:203], a[168:171], v[112:115]\n"
    "buffer_load_dwordx4 v22, s[68:71], s93 offen offset:3072 sc1 lds\n"
    "v_mfma_f32_16x16x32_bf16 v[112:115], a[204:207], a[172:175], v[112:115]\n"
    "v_exp_f32_e32 v100, v100\n"
    "v_mfma_f32_16x16x32_bf16 v[116:119], a[208:211], a[160:163], v[52:55]\n"
    "ds_read_b128 a[244:247], v205 offset:0\n"
    "v_cvt_pk_bf16_f32 v102, v100, v101\n"
    "v_mfma_f32_16x16x32_bf16 v[116:119], a[212:215], a[164:167], v[116:119]\n"
    "v_exp_f32_e32 v99, v99\n"
    "v_mfma_f32_16x16x32_bf16 v[116:119], a[216:219], a[168:171], v[116:119]\n"
    "ds_read_b128 a[248:251], v206 offset:0\n"
    "v_fma_f32 v245, v245, v138, v139\n"
    "v_mfma_f32_16x16x32_bf16 v[116:119], a[220:223], a[172:175], v[116:119]\n"
    "s_add_u32 m0, s92, 0x8000\n"
    "v_exp_f32_e32 v98, v98\n"
    "v_mfma_f32_16x16x32_bf16 v[120:123], a[192:195], a[176:179], v[56:59]\n"
    "ds_read_b128 a[252:255], v207 offset:0\n"
    "v_cvt_pk_bf16_f32 v101, v98, v99\n"
    "v_mfma_f32_16x16x32_bf16 v[120:123], a[196:199], a[180:183], v[120:123]\n"
    "v_exp_f32_e32 v97, v97\n"
    "v_mfma_f32_16x16x32_bf16 v[120:123], a[200:203], a[184:187], v[120:123]\n"
    "buffer_load_dwordx4 v21, s[68:71], s93 offen offset:0 sc1 lds\n"
    "v_mfma_f32_16x16x32_bf16 v[120:123], a[204:207], a[188:191], v[120:123]\n"
    "v_exp_f32_e32 v96, v96\n"
    "v_mfma_f32_16x16x32_bf16 v[124:127], a[208:211], a[176:179], v[56:59]\n"
    "v_cvt_pk_bf16_f32 v100, v96, v97\n"
    "v_fma_f32 v246, v246, v138, v139\n"
    "v_mfma_f32_16x16x32_bf16 v[124:127], a[212:215], a[180:183], v[124:127]\n"
    "v_exp_f32_e32 v104, v104\n"
    "v_mfma_f32_16x16x32_bf16 v[124:127], a[216:219], a[184:187], v[124:127]\n"
    "buffer_load_dwordx4 v21, s[68:71], s93 offen offset:1024 sc1 lds\n"
    "v_mfma_f32_16x16x32_bf16 v[124:127], a[220:223], a[188:191], v[124:127]\n"
    "s_waitcnt lgkmcnt(0)\n"
    "v_exp_f32_e32 v105, v105\n"
    "v_mfma_f32_16x16x32_bf16 a[0:3], v[68:71], a[224:227], a[0:3]\n"
    "v_cvt_pk_bf16_f32 v104, v104, v105\n"
    "v_fma_f32 v247, v247, v138, v139\n"
    "v_mfma_f32_16x16x32_bf16 a[4:7], v[68:71], a[228:231], a[4:7]\n"
    "v_exp_f32_e32 v106, v106\n"
    "v_mfma_f32_16x16x32_bf16 a[8:11], v[68:71], a[232:235], a[8:11]\n"
    "buffer_load_dwordx4 v21, s[68:71], s93 offen offset:2048 sc1 lds\n"
    "v_mfma_f32_16x16x32_bf16 a[12:15], v[68:71], a[236:239], a[12:15]\n"
    "v_exp_f32_e32 v107, v107\n"
    "v_mfma_f32_16x16x32_bf16 a[16:19], v[68:71], a[240:243], a[16:19]\n"
    "ds_read_b128 a[192:195], v16 offset:16384\n"
    "v_cvt_pk_bf16_f32 v105, v106, v107\n"
    "v_mfma_f32_16x16x32_bf16 a[20:23], v[68:71], a[244:247], a[20:23]\n"
    "v_exp_f32_e32 v108, v108\n"
    "v_mfma_f32_16x16x32_bf16 a[24:27], v[68:71], a[248:251], a[24:27]\n"
    "buffer_load_dwordx4 v21, s[68:71], s93 offen offset:3072 sc1 lds\n"
    "v_mfma_f32_16x16x32_bf16 a[28:31], v[68:71], a[252:255], a[28:31]\n"
    "s_add_u32 s93, s93, 0x8000\n"
    "v_exp_f32_e32 v109, v109\n"
    "v_mfma_f32_16x16x32_bf16 a[32:35], v[72:75], a[224:227], a[32:35]\n"
    "ds_read_b128 a[196:199], v17 offset:16384\n"
    "v_cvt_pk_bf16_f32 v106, v108, v109\n"
    "v_mfma_f32_16x16x32_bf16 a[36:39], v[72:75], a[228:231], a[36:39]\n"
    "v_exp_f32_e32 v110, v110\n"
    "v_mfma_f32_16x16x32_bf16 a[40:43], v[72:75], a[232:235], a[40:43]\n"
    "buffer_store_dwordx4 v[188:191], v132, s[68:71], s58 offen sc1\n"
    "v_mfma_f32_16x16x32_bf16 a[44:47], v[72:75], a[236:239], a[44:47]\n"
    "s_add_u32 s58, s58, 0x10000\n"
    "v_exp_f32_e32 v111, v111\n"
    "v_mfma_f32_16x16x32_bf16 a[48:51], v[72:75], a[240:243], a[48:51]\n"
    "ds_read_b128 a[200:203], v18 offset:16384\n"
    "v_cvt_pk_bf16_f32 v107, v110, v111\n"
    "v_mfma_f32_16x16x32_bf16 a[52:55], v[72:75], a[244:247], a[52:55]\n"
    "v_exp_f32_e32 v119, v119\n"
    "v_mfma_f32_16x16x32_bf16 a[56:59], v[72:75], a[248:251], a[56:59]\n"
    "buffer_load_dword v23, v61, s[88:91], 0 offen sc1\n"
    "v_mfma_f32_16x16x32_bf16 a[60:63], v[72:75], a[252:255], a[60:63]\n"
    "v_exp_f32_e32 v118, v118\n"
    "v_smfmac_f32_16x16x64_bf16 v[232:235], v[224:227], v[68:75], v228\n"
    "ds_read_b128 a[204:207], v19 offset:16384\n"
    "v_cvt_pk_bf16_f32 v119, v118, v119\n"
    "v_mfma_f32_16x16x32_bf16 a[64:67], v[84:87], a[224:227], a[64:67]\n"
    "ds_read_b128 a[208:211], v16 offset:20480\n"
    "v_lshlrev_b32_e32 v156, 16, v248\n"
    "v_mfma_f32_16x16x32_bf16 a[68:71], v[84:87], a[228:231], a[68:71]\n"
    "v_exp_f32_e32 v117, v117\n"
    "v_mfma_f32_16x16x32_bf16 a[72:75], v[84:87], a[232:235], a[72:75]\n"
    "ds_read_b128 a[212:215], v17 offset:20480\n"
    "v_and_b32_e32 v157, s95, v248\n"
    "v_mfma_f32_16x16x32_bf16 a[76:79], v[84:87], a[236:239], a[76:79]\n"
    "v_exp_f32_e32 v116, v116\n"
    "v_mfma_f32_16x16x32_bf16 a[80:83], v[84:87], a[240:243], a[80:83]\n"
    "ds_read_b128 a[216:219], v18 offset:20480\n"
    "v_cvt_pk_bf16_f32 v118, v116, v117\n"
    "v_mfma_f32_16x16x32_bf16 a[84:87], v[84:87], a[244:247], a[84:87]\n"
    "v_exp_f32_e32 v115, v115\n"
    "v_mfma_f32_16x16x32_bf16 a[88:91], v[84:87], a[248:251], a[88:91]\n"
    "ds_read_b128 a[220:223], v19 offset:20480\n"
    "v_lshl_add_u32 v158, v252, 16, v137\n"
    "v_mfma_f32_16x16x32_bf16 a[92:95], v[84:87], a[252:255], a[92:95]\n"
    "v_exp_f32_e32 v114, v114\n"
    "v_mfma_f32_16x16x32_bf16 a[96:99], v[88:91], a[224:227], a[96:99]\n"
    "v_cvt_pk_bf16_f32 v117, v114, v115\n"
    "v_bitop3_b32 v159, v252, s95, v137 bitop3:0x6a\n"
    "v_mfma_f32_16x16x32_bf16 a[100:103], v[88:91], a[228:231], a[100:103]\n"
    "v_exp_f32_e32 v113, v113\n"
    "v_mfma_f32_16x16x32_bf16 a[104:107], v[88:91], a[232:235], a[104:107]\n"
    "buffer_load_dwordx4 v[164:167], v130, s[76:79], s43 offen\n"
    "v_mfma_f32_16x16x32_bf16 a[108:111], v[88:91], a[236:239], a[108:111]\n"
    "v_exp_f32_e32 v112, v112\n"
    "v_mfma_f32_16x16x32_bf16 a[112:115], v[88:91], a[240:243], a[112:115]\n"
    "v_cvt_pk_bf16_f32 v116, v112, v113\n"
    "v_mul_f32_e32 v156, v240, v156\n"
    "v_mfma_f32_16x16x32_bf16 a[116:119], v[88:91], a[244:247], a[116:119]\n"
    "v_exp_f32_e32 v120, v120\n"
    "v_mfma_f32_16x16x32_bf16 a[120:123], v[88:91], a[248:251], a[120:123]\n"
    "buffer_load_dwordx4 v[168:171], v130, s[76:79], s43 offen offset:128\n"
    "v_mfma_f32_16x16x32_bf16 a[124:127], v[88:91], a[252:255], a[124:127]\n"
    "s_add_u32 s43, s43, 0x2000\n"
    "v_exp_f32_e32 v121, v121\n"
    "v_smfmac_f32_16x16x64_bf16 v[236:239], v[224:227], v[84:91], v228\n"
    "s_waitcnt lgkmcnt(0)\n"
    "v_cvt_pk_bf16_f32 v120, v120, v121\n"
    "v_mul_f32_e32 v157, v241, v157\n"
    "v_mfma_f32_16x16x32_bf16 v[64:67], a[192:195], a[128:131], v[44:47]\n"
    "v_exp_f32_e32 v122, v122\n"
    "v_mfma_f32_16x16x32_bf16 v[64:67], a[196:199], a[132:135], v[64:67]\n"
    "ds_read_b128 a[224:227], v200 offset:8192\n"
    "v_fmac_f32_dpp v156, v240, v158 quad_perm:[1,0,3,2] row_mask:0xf bank_mask:0xf\n"
    "v_mfma_f32_16x16x32_bf16 v[64:67], a[200:203], a[136:139], v[64:67]\n"
    "v_exp_f32_e32 v123, v123\n"
    "v_mfma_f32_16x16x32_bf16 v[64:67], a[204:207], a[140:143], v[64:67]\n"
    "ds_read_b128 a[228:231], v201 offset:8192\n"
    "v_cvt_pk_bf16_f32 v121, v122, v123\n"
    "v_mfma_f32_16x16x32_bf16 v[68:71], a[208:211], a[128:131], v[44:47]\n"
    "v_exp_f32_e32 v124, v124\n"
    "v_mfma_f32_16x16x32_bf16 v[68:71], a[212:215], a[132:135], v[68:71]\n"
    "ds_read_b128 a[232:235], v202 offset:8192\n"
    "v_fmac_f32_dpp v157, v241, v159 quad_perm:[1,0,3,2] row_mask:0xf bank_mask:0xf\n"
    "v_mfma_f32_16x16x32_bf16 v[68:71], a[216:219], a[136:139], v[68:71]\n"
    "v_exp_f32_e32 v125, v125\n"
    "v_mfma_f32_16x16x32_bf16 v[68:71], a[220:223], a[140:143], v[68:71]\n"
    "ds_read_b128 a[236:239], v203 offset:8192\n"
    "v_cvt_pk_bf16_f32 v122, v124, v125\n"
    "v_cvt_pk_bf16_f32 v160, v156, v157\n"
    "v_mfma_f32_16x16x32_bf16 v[72:75], a[192:195], a[144:147], v[48:51]\n"
    "v_exp_f32_e32 v126, v126\n"
    "v_mfma_f32_16x16x32_bf16 v[72:75], a[196:199], a[148:151], v[72:75]\n"
    "ds_read_b128 a[240:243], v204 offset:8192\n"
    "v_lshlrev_b32_e32 v156, 16, v249\n"
    "v_mfma_f32_16x16x32_bf16 v[72:75], a[200:203], a[152:155], v[72:75]\n"
    "v_exp_f32_e32 v127, v127\n"
    "v_mfma_f32_16x16x32_bf16 v[72:75], a[204:207], a[156:159], v[72:75]\n"
    "ds_read_b128 a[244:247], v205 offset:8192\n"
    "v_cvt_pk_bf16_f32 v123, v126, v127\n"
    "v_mfma_f32_16x16x32_bf16 v[76:79], a[208:211], a[144:147], v[48:51]\n"
    "v_exp_f32_e32 v71, v71\n"
    "v_mfma_f32_16x16x32_bf16 v[76:79], a[212:215], a[148:151], v[76:79]\n"
    "ds_read_b128 a[248:251], v206 offset:8192\n"
    "v_and_b32_e32 v157, s95, v249\n"
    "v_mfma_f32_16x16x32_bf16 v[76:79], a[216:219], a[152:155], v[76:79]\n"
    "v_exp_f32_e32 v70, v70\n"
    "v_mfma_f32_16x16x32_bf16 v[76:79], a[220:223], a[156:159], v[76:79]\n"
    "ds_read_b128 a[252:255], v207 offset:8192\n"
    "v_cvt_pk_bf16_f32 v71, v70, v71\n"
    "v_mfma_f32_16x16x32_bf16 v[80:83], a[192:195], a[160:163], v[52:55]\n"
    "v_lshl_add_u32 v158, v253, 16, v137\n"
    "v_bitop3_b32 v159, v253, s95, v137 bitop3:0x6a\n"
    "v_mfma_f32_16x16x32_bf16 v[80:83], a[196:199], a[164:167], v[80:83]\n"
    "v_exp_f32_e32 v69, v69\n"
    "v_mfma_f32_16x16x32_bf16 v[80:83], a[200:203], a[168:171], v[80:83]\n"
    "v_mul_f32_e32 v156, v242, v156\n"
    "v_mul_f32_e32 v157, v243, v157\n"
    "v_mfma_f32_16x16x32_bf16 v[80:83], a[204:207], a[172:175], v[80:83]\n"
    "v_exp_f32_e32 v68, v68\n"
    "v_mfma_f32_16x16x32_bf16 v[84:87], a[208:211], a[160:163], v[52:55]\n"
    "v_cvt_pk_bf16_f32 v70, v68, v69\n"
    "v_fmac_f32_dpp v156, v242, v158 quad_perm:[1,0,3,2] row_mask:0xf bank_mask:0xf\n"
    "v_mfma_f32_16x16x32_bf16 v[84:87], a[212:215], a[164:167], v[84:87]\n"
    "v_exp_f32_e32 v67, v67\n"
    "v_mfma_f32_16x16x32_bf16 v[84:87], a[216:219], a[168:171], v[84:87]\n"
    "v_fmac_f32_dpp v157, v243, v159 quad_perm:[1,0,3,2] row_mask:0xf bank_mask:0xf\n"
    "v_cvt_pk_bf16_f32 v161, v156, v157\n"
    "v_mfma_f32_16x16x32_bf16 v[84:87], a[220:223], a[172:175], v[84:87]\n"
    "v_exp_f32_e32 v66, v66\n"
    "v_mfma_f32_16x16x32_bf16 v[88:91], a[192:195], a[176:179], v[56:59]\n"
    "v_cvt_pk_bf16_f32 v69, v66, v67\n"
    "v_lshlrev_b32_e32 v156, 16, v250\n"
    "v_mfma_f32_16x16x32_bf16 v[88:91], a[196:199], a[180:183], v[88:91]\n"
    "v_exp_f32_e32 v65, v65\n"
    "v_mfma_f32_16x16x32_bf16 v[88:91], a[200:203], a[184:187], v[88:91]\n"
    "v_and_b32_e32 v157, s95, v250\n"
    "v_lshl_add_u32 v158, v254, 16, v137\n"
    "v_mfma_f32_16x16x32_bf16 v[88:91], a[204:207], a[188:191], v[88:91]\n"
    "v_exp_f32_e32 v64, v64\n"
    "v_mfma_f32_16x16x32_bf16 v[92:95], a[208:211], a[176:179], v[56:59]\n"
    "v_cvt_pk_bf16_f32 v68, v64, v65\n"
    "v_bitop3_b32 v159, v254, s95, v137 bitop3:0x6a\n"
    "v_mfma_f32_16x16x32_bf16 v[92:95], a[212:215], a[180:183], v[92:95]\n"
    "v_exp_f32_e32 v72, v72\n"
    "v_mfma_f32_16x16x32_bf16 v[92:95], a[216:219], a[184:187], v[92:95]\n"
    "v_mul_f32_e32 v156, v244, v156\n"
    "v_mul_f32_e32 v157, v245, v157\n"
    "v_mfma_f32_16x16x32_bf16 v[92:95], a[220:223], a[188:191], v[92:95]\n"
    "s_waitcnt lgkmcnt(0)\n"
    "v_exp_f32_e32 v73, v73\n"
    "v_mfma_f32_16x16x32_bf16 a[0:3], v[100:103], a[224:227], a[0:3]\n"
    "v_cvt_pk_bf16_f32 v72, v72, v73\n"
    "v_fmac_f32_dpp v156, v244, v158 quad_perm:[1,0,3,2] row_mask:0xf bank_mask:0xf\n"
    "v_mfma_f32_16x16x32_bf16 a[4:7], v[100:103], a[228:231], a[4:7]\n"
    "v_exp_f32_e32 v74, v74\n"
    "v_mfma_f32_16x16x32_bf16 a[8:11], v[100:103], a[232:235], a[8:11]\n"
    "ds_read_b128 a[192:195], v16 offset:24576\n"
    "v_fmac_f32_dpp v157, v245, v159 quad_perm:[1,0,3,2] row_mask:0xf bank_mask:0xf\n"
    "v_mfma_f32_16x16x32_bf16 a[12:15], v[100:103], a[236:239], a[12:15]\n"
    "v_exp_f32_e32 v75, v75\n"
    "v_mfma_f32_16x16x32_bf16 a[16:19], v[100:103], a[240:243], a[16:19]\n"
    "ds_read_b128 a[196:199], v17 offset:24576\n"
    "v_cvt_pk_bf16_f32 v73, v74, v75\n"
    "v_mfma_f32_16x16x32_bf16 a[20:23], v[100:103], a[244:247], a[20:23]\n"
    "v_exp_f32_e32 v76, v76\n"
    "v_mfma_f32_16x16x32_bf16 a[24:27], v[100:103], a[248:251], a[24:27]\n"
    "ds_read_b128 a[200:203], v18 offset:24576\n"
    "v_cvt_pk_bf16_f32 v162, v156, v157\n"
    "v_mfma_f32_16x16x32_bf16 a[28:31], v[100:103], a[252:255], a[28:31]\n"
    "v_exp_f32_e32 v77, v77\n"
    "v_mfma_f32_16x16x32_bf16 a[32:35], v[104:107], a[224:227], a[32:35]\n"
    "ds_read_b128 a[204:207], v19 offset:24576\n"
    "v_cvt_pk_bf16_f32 v74, v76, v77\n"
    "v_mfma_f32_16x16x32_bf16 a[36:39], v[104:107], a[228:231], a[36:39]\n"
    "v_exp_f32_e32 v78, v78\n"
    "v_mfma_f32_16x16x32_bf16 a[40:43], v[104:107], a[232:235], a[40:43]\n"
    "ds_read_b128 a[208:211], v16 offset:28672\n"
    "v_lshlrev_b32_e32 v156, 16, v251\n"
    "v_mfma_f32_16x16x32_bf16 a[44:47], v[104:107], a[236:239], a[44:47]\n"
    "v_exp_f32_e32 v79, v79\n"
    "v_mfma_f32_16x16x32_bf16 a[48:51], v[104:107], a[240:243], a[48:51]\n"
    "ds_read_b128 a[212:215], v17 offset:28672\n"
    "v_cvt_pk_bf16_f32 v75, v78, v79\n"
    "v_mfma_f32_16x16x32_bf16 a[52:55], v[104:107], a[244:247], a[52:55]\n"
    "v_exp_f32_e32 v87, v87\n"
    "v_mfma_f32_16x16x32_bf16 a[56:59], v[104:107], a[248:251], a[56:59]\n"
    "s_waitcnt vmcnt(2)\n"
    "ds_read_b128 a[216:219], v18 offset:28672\n"
    "v_max_u32_e32 v23, s96, v23\n"
    "v_mfma_f32_16x16x32_bf16 a[60:63], v[104:107], a[252:255], a[60:63]\n"
    "v_exp_f32_e32 v86, v86\n"
    "v_smfmac_f32_16x16x64_bf16 v[232:235], v[224:227], v[100:107], v228\n"
    "ds_write_b32 v229, v23\n"
    "v_cvt_pk_bf16_f32 v87, v86, v87\n"
    "v_mfma_f32_16x16x32_bf16 a[64:67], v[116:119], a[224:227], a[64:67]\n"
    "ds_read_b128 a[220:223], v19 offset:28672\n"
    "v_and_b32_e32 v157, s95, v251\n"
    "v_mfma_f32_16x16x32_bf16 a[68:71], v[116:119], a[228:231], a[68:71]\n"
    "v_exp_f32_e32 v85, v85\n"
    "v_mfma_f32_16x16x32_bf16 a[72:75], v[116:119], a[232:235], a[72:75]\n"
    "ds_read_b128 v[180:183], v134\n"
    "v_lshl_add_u32 v158, v255, 16, v137\n"
    "v_mfma_f32_16x16x32_bf16 a[76:79], v[116:119], a[236:239], a[76:79]\n"
    "v_exp_f32_e32 v84, v84\n"
    "v_mfma_f32_16x16x32_bf16 a[80:83], v[116:119], a[240:243], a[80:83]\n"
    "v_cvt_pk_bf16_f32 v86, v84, v85\n"
    "v_add_u32_e32 v134, 0x200, v134\n"
    "v_mfma_f32_16x16x32_bf16 a[84:87], v[116:119], a[244:247], a[84:87]\n"
    "v_exp_f32_e32 v83, v83\n"
    "v_mfma_f32_16x16x32_bf16 a[88:91], v[116:119], a[248:251], a[88:91]\n"
    "ds_read_b32 v231, v230\n"
    "v_mov_b32_e32 v62, s60\n"
    "v_mfma_f32_16x16x32_bf16 a[92:95], v[116:119], a[252:255], a[92:95]\n"
    "v_exp_f32_e32 v82, v82\n"
    "v_mfma_f32_16x16x32_bf16 a[96:99], v[120:123], a[224:227], a[96:99]\n"
    "v_cvt_pk_bf16_f32 v85, v82, v83\n"
    "v_bitop3_b32 v159, v255, s95, v137 bitop3:0x6a\n"
    "v_mfma_f32_16x16x32_bf16 a[100:103], v[120:123], a[228:231], a[100:103]\n"
    "v_exp_f32_e32 v81, v81\n"
    "v_mfma_f32_16x16x32_bf16 a[104:107], v[120:123], a[232:235], a[104:107]\n"
    "v_mul_f32_e32 v156, v246, v156\n"
    "v_mul_f32_e32 v157, v247, v157\n"
    "v_mfma_f32_16x16x32_bf16 a[108:111], v[120:123], a[236:239], a[108:111]\n"
    "v_exp_f32_e32 v80, v80\n"
    "v_mfma_f32_16x16x32_bf16 a[112:115], v[120:123], a[240:243], a[112:115]\n"
    "v_cvt_pk_bf16_f32 v84, v80, v81\n"
    "v_fmac_f32_dpp v156, v246, v158 quad_perm:[1,0,3,2] row_mask:0xf bank_mask:0xf\n"
    "v_mfma_f32_16x16x32_bf16 a[116:119], v[120:123], a[244:247], a[116:119]\n"
    "v_exp_f32_e32 v88, v88\n"
    "v_mfma_f32_16x16x32_bf16 a[120:123], v[120:123], a[248:251], a[120:123]\n"
    "s_waitcnt lgkmcnt(0)\n"
    "v_cmp_lt_u32_e32 vcc, s96, v231\n"
    "v_fmac_f32_dpp v157, v247, v159 quad_perm:[1,0,3,2] row_mask:0xf bank_mask:0xf\n"
    "v_mfma_f32_16x16x32_bf16 a[124:127], v[120:123], a[252:255], a[124:127]\n"
    "v_exp_f32_e32 v89, v89\n"
    "v_smfmac_f32_16x16x64_bf16 v[236:239], v[224:227], v[116:123], v228\n"
    "s_cbranch_vccnz .Lspin_l0\n"
    ".Lback_l0:\n"
    "v_cvt_pk_bf16_f32 v88, v88, v89\n"
    "v_cvt_pk_bf16_f32 v163, v156, v157\n"
    "v_mfma_f32_16x16x32_bf16 v[96:99], a[192:195], a[128:131], v[44:47]\n"
    "v_exp_f32_e32 v90, v90\n"
    "v_mfma_f32_16x16x32_bf16 v[96:99], a[196:199], a[132:135], v[96:99]\n"
    "buffer_store_dword v62, v60, s[88:91], 0 offen sc1\n"
    "v_mfma_f32_16x16x32_bf16 v[96:99], a[200:203], a[136:139], v[96:99]\n"
    "s_add_u32 m0, s92, 0x18000\n"
    "v_exp_f32_e32 v91, v91\n"
    "v_mfma_f32_16x16x32_bf16 v[96:99], a[204:207], a[140:143], v[96:99]\n"
    "s_sub_u32 s96, s96, 1\n"
    "ds_read_b128 a[224:227], v200 offset:16384\n"
    "v_cvt_pk_bf16_f32 v89, v90, v91\n"
    "v_mfma_f32_16x16x32_bf16 v[100:103], a[208:211], a[128:131], v[44:47]\n"
    "s_sub_u32 s60, s60, 1\n"
    "v_exp_f32_e32 v92, v92\n"
    "v_mfma_f32_16x16x32_bf16 v[100:103], a[212:215], a[132:135], v[100:103]\n"
    "buffer_load_dwordx4 v22, s[68:71], s93 offen offset:0 sc1 lds\n"
    "v_mfma_f32_16x16x32_bf16 v[100:103], a[216:219], a[136:139], v[100:103]\n"
    "v_exp_f32_e32 v93, v93\n"
    "v_mfma_f32_16x16x32_bf16 v[100:103], a[220:223], a[140:143], v[100:103]\n"
    "ds_read_b128 a[228:231], v201 offset:16384\n"
    "v_cvt_pk_bf16_f32 v90, v92, v93\n"
    "v_mfma_f32_16x16x32_bf16 v[104:107], a[192:195], a[144:147], v[48:51]\n"
    "v_exp_f32_e32 v94, v94\n"
    "v_mfma_f32_16x16x32_bf16 v[104:107], a[196:199], a[148:151], v[104:107]\n"
    "buffer_load_dwordx4 v22, s[68:71], s93 offen offset:1024 sc1 lds\n"
    "v_mfma_f32_16x16x32_bf16 v[104:107], a[200:203], a[152:155], v[104:107]\n"
    "v_exp_f32_e32 v95, v95\n"
    "v_mfma_f32_16x16x32_bf16 v[104:107], a[204:207], a[156:159], v[104:107]\n"
    "ds_read_b128 a[232:235], v202 offset:16384\n"
    "v_cvt_pk_bf16_f32 v91, v94, v95\n"
    "v_mfma_f32_16x16x32_bf16 v[108:111], a[208:211], a[144:147], v[48:51]\n"
    "v_exp_f32_e32 v103, v103\n"
    "v_mfma_f32_16x16x32_bf16 v[108:111], a[212:215], a[148:151], v[108:111]\n"
    "s_waitcnt vmcnt(20)\n"
    "ds_read_b128 a[236:239], v203 offset:16384\n"
    "v_cvt_f32_i32_e32 v172, v172\n"
    "v_mfma_f32_16x16x32_bf16 v[108:111], a[216:219], a[152:155], v[108:111]\n"
    "v_exp_f32_e32 v102, v102\n"
    "v_mfma_f32_16x16x32_bf16 v[108:111], a[220:223], a[156:159], v[108:111]\n"
    "ds_read_b128 a[240:243], v204 offset:16384\n"
    "v_cvt_pk_bf16_f32 v103, v102, v103\n"
    "v_mfma_f32_16x16x32_bf16 v[112:115], a[192:195], a[160:163], v[52:55]\n"
    "buffer_load_dwordx4 v22, s[68:71], s93 offen offset:2048 sc1 lds\n"
    "v_mfma_f32_16x16x32_bf16 v[112:115], a[196:199], a[164:167], v[112:115]\n"
    "v_exp_f32_e32 v101, v101\n"
    "v_mfma_f32_16x16x32_bf16 v[112:115], a[200:203], a[168:171], v[112:115]\n"
    "buffer_load_dwordx4 v22, s[68:71], s93 offen offset:3072 sc1 lds\n"
    "v_mfma_f32_16x16x32_bf16 v[112:115], a[204:207], a[172:175], v[112:115]\n"
    "v_exp_f32_e32 v100, v100\n"
    "v_mfma_f32_16x16x32_bf16 v[116:119], a[208:211], a[160:163], v[52:55]\n"
    "ds_read_b128 a[244:247], v205 offset:16384\n"
    "v_cvt_pk_bf16_f32 v102, v100, v101\n"
    "v_mfma_f32_16x16x32_bf16 v[116:119], a[212:215], a[164:167], v[116:119]\n"
    "v_exp_f32_e32 v99, v99\n"
    "v_mfma_f32_16x16x32_bf16 v[116:119], a[216:219], a[168:171], v[116:119]\n"
    "ds_read_b128 a[248:251], v206 offset:16384\n"
    "v_cvt_f32_i32_e32 v173, v173\n"
    "v_mfma_f32_16x16x32_bf16 v[116:119], a[220:223], a[172:175], v[116:119]\n"
    "s_add_u32 m0, s92, 0xc000\n"
    "v_exp_f32_e32 v98, v98\n"
    "v_mfma_f32_16x16x32_bf16 v[120:123], a[192:195], a[176:179], v[56:59]\n"
    "ds_read_b128 a[252:255], v207 offset:16384\n"
    "v_cvt_pk_bf16_f32 v101, v98, v99\n"
    "v_mfma_f32_16x16x32_bf16 v[120:123], a[196:199], a[180:183], v[120:123]\n"
    "v_exp_f32_e32 v97, v97\n"
    "v_mfma_f32_16x16x32_bf16 v[120:123], a[200:203], a[184:187], v[120:123]\n"
    "buffer_load_dwordx4 v21, s[68:71], s93 offen offset:0 sc1 lds\n"
    "v_mfma_f32_16x16x32_bf16 v[120:123], a[204:207], a[188:191], v[120:123]\n"
    "v_exp_f32_e32 v96, v96\n"
    "v_mfma_f32_16x16x32_bf16 v[124:127], a[208:211], a[176:179], v[56:59]\n"
    "v_cvt_pk_bf16_f32 v100, v96, v97\n"
    "v_cvt_f32_i32_e32 v174, v174\n"
    "v_mfma_f32_16x16x32_bf16 v[124:127], a[212:215], a[180:183], v[124:127]\n"
    "v_exp_f32_e32 v104, v104\n"
    "v_mfma_f32_16x16x32_bf16 v[124:127], a[216:219], a[184:187], v[124:127]\n"
    "buffer_load_dwordx4 v21, s[68:71], s93 offen offset:1024 sc1 lds\n"
    "v_mfma_f32_16x16x32_bf16 v[124:127], a[220:223], a[188:191], v[124:127]\n"
    "s_waitcnt lgkmcnt(0)\n"
    "v_exp_f32_e32 v105, v105\n"
    "v_mfma_f32_16x16x32_bf16 a[0:3], v[68:71], a[224:227], a[0:3]\n"
    "v_cvt_pk_bf16_f32 v104, v104, v105\n"
    "v_cvt_f32_i32_e32 v175, v175\n"
    "v_mfma_f32_16x16x32_bf16 a[4:7], v[68:71], a[228:231], a[4:7]\n"
    "v_exp_f32_e32 v106, v106\n"
    "v_mfma_f32_16x16x32_bf16 a[8:11], v[68:71], a[232:235], a[8:11]\n"
    "buffer_load_dwordx4 v21, s[68:71], s93 offen offset:2048 sc1 lds\n"
    "v_mfma_f32_16x16x32_bf16 a[12:15], v[68:71], a[236:239], a[12:15]\n"
    "v_exp_f32_e32 v107, v107\n"
    "v_mfma_f32_16x16x32_bf16 a[16:19], v[68:71], a[240:243], a[16:19]\n"
    "ds_read_b128 a[192:195], v16 offset:32768\n"
    "v_cvt_pk_bf16_f32 v105, v106, v107\n"
    "v_mfma_f32_16x16x32_bf16 a[20:23], v[68:71], a[244:247], a[20:23]\n"
    "v_exp_f32_e32 v108, v108\n"
    "v_mfma_f32_16x16x32_bf16 a[24:27], v[68:71], a[248:251], a[24:27]\n"
    "buffer_load_dwordx4 v21, s[68:71], s93 offen offset:3072 sc1 lds\n"
    "v_mfma_f32_16x16x32_bf16 a[28:31], v[68:71], a[252:255], a[28:31]\n"
    "s_add_u32 s93, s93, 0x8000\n"
    "v_exp_f32_e32 v109, v109\n"
    "v_mfma_f32_16x16x32_bf16 a[32:35], v[72:75], a[224:227], a[32:35]\n"
    "ds_read_b128 a[196:199], v17 offset:32768\n"
    "v_cvt_pk_bf16_f32 v106, v108, v109\n"
    "v_mfma_f32_16x16x32_bf16 a[36:39], v[72:75], a[228:231], a[36:39]\n"
    "v_exp_f32_e32 v110, v110\n"
    "v_mfma_f32_16x16x32_bf16 a[40:43], v[72:75], a[232:235], a[40:43]\n"
    "buffer_store_dwordx4 v[160:163], v131, s[68:71], s49 offen sc1\n"
    "v_mfma_f32_16x16x32_bf16 a[44:47], v[72:75], a[236:239], a[44:47]\n"
    "s_add_u32 s49, s49, 0x10000\n"
    "v_exp_f32_e32 v111, v111\n"
    "v_mfma_f32_16x16x32_bf16 a[48:51], v[72:75], a[240:243], a[48:51]\n"
    "ds_read_b128 a[200:203], v18 offset:32768\n"
    "v_cvt_pk_bf16_f32 v107, v110, v111\n"
    "v_mfma_f32_16x16x32_bf16 a[52:55], v[72:75], a[244:247], a[52:55]\n"
    "v_exp_f32_e32 v119, v119\n"
    "v_mfma_f32_16x16x32_bf16 a[56:59], v[72:75], a[248:251], a[56:59]\n"
    "buffer_load_dword v23, v61, s[88:91], 0 offen sc1\n"
    "v_mfma_f32_16x16x32_bf16 a[60:63], v[72:75], a[252:255], a[60:63]\n"
    "v_exp_f32_e32 v118, v118\n"
    "v_smfmac_f32_16x16x64_bf16 v[232:235], v[224:227], v[68:75], v228\n"
    "ds_read_b128 a[204:207], v19 offset:32768\n"
    "v_cvt_pk_bf16_f32 v119, v118, v119\n"
    "v_mfma_f32_16x16x32_bf16 a[64:67], v[84:87], a[224:227], a[64:67]\n"
    "ds_read_b128 a[208:211], v16 offset:36864\n"
    "v_cvt_f32_i32_e32 v176, v176\n"
    "v_mfma_f32_16x16x32_bf16 a[68:71], v[84:87], a[228:231], a[68:71]\n"
    "v_exp_f32_e32 v117, v117\n"
    "v_mfma_f32_16x16x32_bf16 a[72:75], v[84:87], a[232:235], a[72:75]\n"
    "ds_read_b128 a[212:215], v17 offset:36864\n"
    "v_cvt_f32_i32_e32 v177, v177\n"
    "v_mfma_f32_16x16x32_bf16 a[76:79], v[84:87], a[236:239], a[76:79]\n"
    "v_exp_f32_e32 v116, v116\n"
    "v_mfma_f32_16x16x32_bf16 a[80:83], v[84:87], a[240:243], a[80:83]\n"
    "ds_read_b128 a[216:219], v18 offset:36864\n"
    "v_cvt_pk_bf16_f32 v118, v116, v117\n"
    "v_mfma_f32_16x16x32_bf16 a[84:87], v[84:87], a[244:247], a[84:87]\n"
    "v_exp_f32_e32 v115, v115\n"
    "v_mfma_f32_16x16x32_bf16 a[88:91], v[84:87], a[248:251], a[88:91]\n"
    "ds_read_b128 a[220:223], v19 offset:36864\n"
    "v_cvt_f32_i32_e32 v178, v178\n"
    "v_mfma_f32_16x16x32_bf16 a[92:95], v[84:87], a[252:255], a[92:95]\n"
    "v_exp_f32_e32 v114, v114\n"
    "v_mfma_f32_16x16x32_bf16 a[96:99], v[88:91], a[224:227], a[96:99]\n"
    "v_cvt_pk_bf16_f32 v117, v114, v115\n"
    "v_cvt_f32_i32_e32 v179, v179\n"
    "v_mfma_f32_16x16x32_bf16 a[100:103], v[88:91], a[228:231], a[100:103]\n"
    "v_exp_f32_e32 v113, v113\n"
    "v_mfma_f32_16x16x32_bf16 a[104:107], v[88:91], a[232:235], a[104:107]\n"
    "buffer_load_dwordx4 v[240:243], v128, s[72:75], s42 offen\n"
    "v_mfma_f32_16x16x32_bf16 a[108:111], v[88:91], a[236:239], a[108:111]\n"
    "v_exp_f32_e32 v112, v112\n"
    "v_mfma_f32_16x16x32_bf16 a[112:115], v[88:91], a[240:243], a[112:115]\n"
    "v_cvt_pk_bf16_f32 v116, v112, v113\n"
    "v_fma_f32 v172, v172, v180, v181\n"
    "v_mfma_f32_16x16x32_bf16 a[116:119], v[88:91], a[244:247], a[116:119]\n"
    "v_exp_f32_e32 v120, v120\n"
    "v_mfma_f32_16x16x32_bf16 a[120:123], v[88:91], a[248:251], a[120:123]\n"
    "buffer_load_dwordx4 v[244:247], v128, s[72:75], s42 offen offset:16\n"
    "v_mfma_f32_16x16x32_bf16 a[124:127], v[88:91], a[252:255], a[124:127]\n"
    "s_add_u32 s42, s42, 0x2000\n"
    "v_exp_f32_e32 v121, v121\n"
    "v_smfmac_f32_16x16x64_bf16 v[236:239], v[224:227], v[84:91], v228\n"
    "s_waitcnt lgkmcnt(0)\n"
    "s_min_u32 s42, s42, 0x1e000\n"
    "v_cvt_pk_bf16_f32 v120, v120, v121\n"
    "v_fma_f32 v173, v173, v180, v181\n"
    "v_mfma_f32_16x16x32_bf16 v[64:67], a[192:195], a[128:131], v[44:47]\n"
    "v_exp_f32_e32 v122, v122\n"
    "v_mfma_f32_16x16x32_bf16 v[64:67], a[196:199], a[132:135], v[64:67]\n"
    "buffer_load_dwordx4 v[248:251], v129, s[80:83], s48 offen\n"
    "v_mfma_f32_16x16x32_bf16 v[64:67], a[200:203], a[136:139], v[64:67]\n"
    "v_exp_f32_e32 v123, v123\n"
    "v_mfma_f32_16x16x32_bf16 v[64:67], a[204:207], a[140:143], v[64:67]\n"
    "ds_read_b128 a[224:227], v200 offset:24576\n"
    "v_cvt_pk_bf16_f32 v121, v122, v123\n"
    "v_mfma_f32_16x16x32_bf16 v[68:71], a[208:211], a[128:131], v[44:47]\n"
    "v_exp_f32_e32 v124, v124\n"
    "v_mfma_f32_16x16x32_bf16 v[68:71], a[212:215], a[132:135], v[68:71]\n"
    "buffer_load_dwordx4 v[252:255], v129, s[84:87], s48 offen\n"
    "v_mfma_f32_16x16x32_bf16 v[68:71], a[216:219], a[136:139], v[68:71]\n"
    "s_add_u32 s48, s48, 0x1000\n"
    "v_exp_f32_e32 v125, v125\n"
    "v_mfma_f32_16x16x32_bf16 v[68:71], a[220:223], a[140:143], v[68:71]\n"
    "s_min_u32 s48, s48, 0xf000\n"
    "ds_read_b128 a[228:231], v201 offset:24576\n"
    "v_cvt_pk_bf16_f32 v122, v124, v125\n"
    "v_mfma_f32_16x16x32_bf16 v[72:75], a[192:195], a[144:147], v[48:51]\n"
    "v_exp_f32_e32 v126, v126\n"
    "v_mfma_f32_16x16x32_bf16 v[72:75], a[196:199], a[148:151], v[72:75]\n"
    "ds_read_b128 a[232:235], v202 offset:24576\n"
    "v_fma_f32 v174, v174, v180, v181\n"
    "v_mfma_f32_16x16x32_bf16 v[72:75], a[200:203], a[152:155], v[72:75]\n"
    "v_exp_f32_e32 v127, v127\n"
    "v_mfma_f32_16x16x32_bf16 v[72:75], a[204:207], a[156:159], v[72:75]\n"
    "ds_read_b128 a[236:239], v203 offset:24576\n"
    "v_cvt_pk_bf16_f32 v123, v126, v127\n"
    "v_mfma_f32_16x16x32_bf16 v[76:79], a[208:211], a[144:147], v[48:51]\n"
    "v_exp_f32_e32 v71, v71\n"
    "v_mfma_f32_16x16x32_bf16 v[76:79], a[212:215], a[148:151], v[76:79]\n"
    "ds_read_b128 a[240:243], v204 offset:24576\n"
    "v_fma_f32 v175, v175, v180, v181\n"
    "v_mfma_f32_16x16x32_bf16 v[76:79], a[216:219], a[152:155], v[76:79]\n"
    "v_exp_f32_e32 v70, v70\n"
    "v_mfma_f32_16x16x32_bf16 v[76:79], a[220:223], a[156:159], v[76:79]\n"
    "ds_read_b128 a[244:247], v205 offset:24576\n"
    "v_cvt_pk_bf16_f32 v71, v70, v71\n"
    "v_mfma_f32_16x16x32_bf16 v[80:83], a[192:195], a[160:163], v[52:55]\n"
    "ds_read_b128 a[248:251], v206 offset:24576\n"
    "v_fma_f32 v176, v176, v182, v183\n"
    "v_mfma_f32_16x16x32_bf16 v[80:83], a[196:199], a[164:167], v[80:83]\n"
    "v_exp_f32_e32 v69, v69\n"
    "v_mfma_f32_16x16x32_bf16 v[80:83], a[200:203], a[168:171], v[80:83]\n"
    "ds_read_b128 a[252:255], v207 offset:24576\n"
    "v_fma_f32 v177, v177, v182, v183\n"
    "v_mfma_f32_16x16x32_bf16 v[80:83], a[204:207], a[172:175], v[80:83]\n"
    "v_exp_f32_e32 v68, v68\n"
    "v_mfma_f32_16x16x32_bf16 v[84:87], a[208:211], a[160:163], v[52:55]\n"
    "ds_read_b64 v[138:139], v133\n"
    "v_cvt_pk_bf16_f32 v70, v68, v69\n"
    "v_mfma_f32_16x16x32_bf16 v[84:87], a[212:215], a[164:167], v[84:87]\n"
    "v_exp_f32_e32 v67, v67\n"
    "v_mfma_f32_16x16x32_bf16 v[84:87], a[216:219], a[168:171], v[84:87]\n"
    "v_add_u32_e32 v133, 0x200, v133\n"
    "v_fma_f32 v178, v178, v182, v183\n"
    "v_mfma_f32_16x16x32_bf16 v[84:87], a[220:223], a[172:175], v[84:87]\n"
    "v_exp_f32_e32 v66, v66\n"
    "v_mfma_f32_16x16x32_bf16 v[88:91], a[192:195], a[176:179], v[56:59]\n"
    "v_cvt_pk_bf16_f32 v69, v66, v67\n"
    "v_fma_f32 v179, v179, v182, v183\n"
    "v_mfma_f32_16x16x32_bf16 v[88:91], a[196:199], a[180:183], v[88:91]\n"
    "v_exp_f32_e32 v65, v65\n"
    "v_mfma_f32_16x16x32_bf16 v[88:91], a[200:203], a[184:187], v[88:91]\n"
    "v_cvt_pk_bf16_f32 v184, v172, v173\n"
    "v_cvt_pk_bf16_f32 v185, v174, v175\n"
    "v_mfma_f32_16x16x32_bf16 v[88:91], a[204:207], a[188:191], v[88:91]\n"
    "v_exp_f32_e32 v64, v64\n"
    "v_mfma_f32_16x16x32_bf16 v[92:95], a[208:211], a[176:179], v[56:59]\n"
    "v_cvt_pk_bf16_f32 v68, v64, v65\n"
    "v_cvt_pk_bf16_f32 v186, v176, v177\n"
    "v_mfma_f32_16x16x32_bf16 v[92:95], a[212:215], a[180:183], v[92:95]\n"
    "v_exp_f32_e32 v72, v72\n"
    "v_mfma_f32_16x16x32_bf16 v[92:95], a[216:219], a[184:187], v[92:95]\n"
    "v_cvt_pk_bf16_f32 v187, v178, v179\n"
    "v_mfma_f32_16x16x32_bf16 v[92:95], a[220:223], a[188:191], v[92:95]\n"
    "s_waitcnt lgkmcnt(0)\n"
    "v_exp_f32_e32 v73, v73\n"
    "v_mfma_f32_16x16x32_bf16 a[0:3], v[100:103], a[224:227], a[0:3]\n"
    "ds_write_b128 v135, v[184:187]\n"
    "v_cvt_pk_bf16_f32 v72, v72, v73\n"
    "v_mfma_f32_16x16x32_bf16 a[4:7], v[100:103], a[228:231], a[4:7]\n"
    "v_exp_f32_e32 v74, v74\n"
    "v_mfma_f32_16x16x32_bf16 a[8:11], v[100:103], a[232:235], a[8:11]\n"
    "s_waitcnt vmcnt(28)\n"
    "ds_read_b128 a[192:195], v16 offset:40960\n"
    "v_cvt_f32_i32_e32 v140, v140\n"
    "v_mfma_f32_16x16x32_bf16 a[12:15], v[100:103], a[236:239], a[12:15]\n"
    "v_exp_f32_e32 v75, v75\n"
    "v_mfma_f32_16x16x32_bf16 a[16:19], v[100:103], a[240:243], a[16:19]\n"
    "ds_read_b128 a[196:199], v17 offset:40960\n"
    "v_cvt_pk_bf16_f32 v73, v74, v75\n"
    "v_mfma_f32_16x16x32_bf16 a[20:23], v[100:103], a[244:247], a[20:23]\n"
    "v_exp_f32_e32 v76, v76\n"
    "v_mfma_f32_16x16x32_bf16 a[24:27], v[100:103], a[248:251], a[24:27]\n"
    "ds_read_b128 a[200:203], v18 offset:40960\n"
    "v_cvt_f32_i32_e32 v141, v141\n"
    "v_mfma_f32_16x16x32_bf16 a[28:31], v[100:103], a[252:255], a[28:31]\n"
    "v_exp_f32_e32 v77, v77\n"
    "v_mfma_f32_16x16x32_bf16 a[32:35], v[104:107], a[224:227], a[32:35]\n"
    "ds_read_b128 a[204:207], v19 offset:40960\n"
    "v_cvt_pk_bf16_f32 v74, v76, v77\n"
    "v_mfma_f32_16x16x32_bf16 a[36:39], v[104:107], a[228:231], a[36:39]\n"
    "v_exp_f32_e32 v78, v78\n"
    "v_mfma_f32_16x16x32_bf16 a[40:43], v[104:107], a[232:235], a[40:43]\n"
    "ds_read_b128 a[208:211], v16 offset:45056\n"
    "v_cvt_f32_i32_e32 v142, v142\n"
    "v_mfma_f32_16x16x32_bf16 a[44:47], v[104:107], a[236:239], a[44:47]\n"
    "v_exp_f32_e32 v79, v79\n"
    "v_mfma_f32_16x16x32_bf16 a[48:51], v[104:107], a[240:243], a[48:51]\n"
    "ds_read_b128 a[212:215], v17 offset:45056\n"
    "v_cvt_pk_bf16_f32 v75, v78, v79\n"
    "v_mfma_f32_16x16x32_bf16 a[52:55], v[104:107], a[244:247], a[52:55]\n"
    "v_exp_f32_e32 v87, v87\n"
    "v_mfma_f32_16x16x32_bf16 a[56:59], v[104:107], a[248:251], a[56:59]\n"
    "s_waitcnt vmcnt(4)\n"
    "ds_read_b128 a[216:219], v18 offset:45056\n"
    "v_max_u32_e32 v23, s96, v23\n"
    "v_mfma_f32_16x16x32_bf16 a[60:63], v[104:107], a[252:255], a[60:63]\n"
    "v_exp_f32_e32 v86, v86\n"
    "v_smfmac_f32_16x16x64_bf16 v[232:235], v[224:227], v[100:107], v228\n"
    "ds_write_b32 v229, v23\n"
    "v_cvt_pk_bf16_f32 v87, v86, v87\n"
    "v_mfma_f32_16x16x32_bf16 a[64:67], v[116:119], a[224:227], a[64:67]\n"
    "ds_read_b128 a[220:223], v19 offset:45056\n"
    "v_cvt_f32_i32_e32 v143, v143\n"
    "v_cvt_f32_i32_e32 v144, v144\n"
    "v_mfma_f32_16x16x32_bf16 a[68:71], v[116:119], a[228:231], a[68:71]\n"
    "v_exp_f32_e32 v85, v85\n"
    "v_mfma_f32_16x16x32_bf16 a[72:75], v[116:119], a[232:235], a[72:75]\n"
    "ds_read_b64_tr_b16 v[188:189], v136\n"
    "v_cvt_f32_i32_e32 v145, v145\n"
    "v_mfma_f32_16x16x32_bf16 a[76:79], v[116:119], a[236:239], a[76:79]\n"
    "v_exp_f32_e32 v84, v84\n"
    "v_mfma_f32_16x16x32_bf16 a[80:83], v[116:119], a[240:243], a[80:83]\n"
    "ds_read_b64_tr_b16 v[190:191], v136 offset:512\n"
    "v_cvt_pk_bf16_f32 v86, v84, v85\n"
    "v_mfma_f32_16x16x32_bf16 a[84:87], v[116:119], a[244:247], a[84:87]\n"
    "v_exp_f32_e32 v83, v83\n"
    "v_mfma_f32_16x16x32_bf16 a[88:91], v[116:119], a[248:251], a[88:91]\n"
    "ds_read_b32 v231, v230\n"
    "v_mov_b32_e32 v62, s60\n"
    "v_mfma_f32_16x16x32_bf16 a[92:95], v[116:119], a[252:255], a[92:95]\n"
    "v_exp_f32_e32 v82, v82\n"
    "v_mfma_f32_16x16x32_bf16 a[96:99], v[120:123], a[224:227], a[96:99]\n"
    "v_cvt_pk_bf16_f32 v85, v82, v83\n"
    "v_cvt_f32_i32_e32 v146, v146\n"
    "v_mfma_f32_16x16x32_bf16 a[100:103], v[120:123], a[228:231], a[100:103]\n"
    "v_exp_f32_e32 v81, v81\n"
    "v_mfma_f32_16x16x32_bf16 a[104:107], v[120:123], a[232:235], a[104:107]\n"
    "v_cvt_f32_i32_e32 v147, v147\n"
    "v_fma_f32 v140, v140, v138, v139\n"
    "v_mfma_f32_16x16x32_bf16 a[108:111], v[120:123], a[236:239], a[108:111]\n"
    "v_exp_f32_e32 v80, v80\n"
    "v_mfma_f32_16x16x32_bf16 a[112:115], v[120:123], a[240:243], a[112:115]\n"
    "v_cvt_pk_bf16_f32 v84, v80, v81\n"
    "v_fma_f32 v141, v141, v138, v139\n"
    "v_mfma_f32_16x16x32_bf16 a[116:119], v[120:123], a[244:247], a[116:119]\n"
    "v_exp_f32_e32 v88, v88\n"
    "v_mfma_f32_16x16x32_bf16 a[120:123], v[120:123], a[248:251], a[120:123]\n"
    "s_waitcnt lgkmcnt(0)\n"
    "v_cmp_lt_u32_e32 vcc, s96, v231\n"
    "v_fma_f32 v142, v142, v138, v139\n"
    "v_mfma_f32_16x16x32_bf16 a[124:127], v[120:123], a[252:255], a[124:127]\n"
    "v_exp_f32_e32 v89, v89\n"
    "v_smfmac_f32_16x16x64_bf16 v[236:239], v[224:227], v[116:123], v228\n"
    "s_cbranch_vccnz .Lspin_l1\n"
    ".Lback_l1:\n"
    "v_cvt_pk_bf16_f32 v88, v88, v89\n"
    "v_fma_f32 v143, v143, v138, v139\n"
    "v_mfma_f32_16x16x32_bf16 v[96:99], a[192:195], a[128:131], v[44:47]\n"
    "v_exp_f32_e32 v90, v90\n"
    "v_mfma_f32_16x16x32_bf16 v[96:99], a[196:199], a[132:135], v[96:99]\n"
    "buffer_store_dword v62, v60, s[88:91], 0 offen sc1\n"
    "v_mfma_f32_16x16x32_bf16 v[96:99], a[200:203], a[136:139], v[96:99]\n"
    "s_add_u32 m0, s92, 0x1c000\n"
    "v_exp_f32_e32 v91, v91\n"
    "v_mfma_f32_16x16x32_bf16 v[96:99], a[204:207], a[140:143], v[96:99]\n"
    "s_sub_u32 s96, s96, 1\n"
    "ds_read_b128 a[224:227], v200 offset:32768\n"
    "v_cvt_pk_bf16_f32 v89, v90, v91\n"
    "v_mfma_f32_16x16x32_bf16 v[100:103], a[208:211], a[128:131], v[44:47]\n"
    "s_sub_u32 s60, s60, 1\n"
    "v_exp_f32_e32 v92, v92\n"
    "v_mfma_f32_16x16x32_bf16 v[100:103], a[212:215], a[132:135], v[100:103]\n"
    "buffer_load_dwordx4 v22, s[68:71], s93 offen offset:0 sc1 lds\n"
    "v_mfma_f32_16x16x32_bf16 v[100:103], a[216:219], a[136:139], v[100:103]\n"
    "v_exp_f32_e32 v93, v93\n"
    "v_mfma_f32_16x16x32_bf16 v[100:103], a[220:223], a[140:143], v[100:103]\n"
    "ds_read_b128 a[228:231], v201 offset:32768\n"
    "v_cvt_pk_bf16_f32 v90, v92, v93\n"
    "v_mfma_f32_16x16x32_bf16 v[104:107], a[192:195], a[144:147], v[48:51]\n"
    "v_exp_f32_e32 v94, v94\n"
    "v_mfma_f32_16x16x32_bf16 v[104:107], a[196:199], a[148:151], v[104:107]\n"
    "buffer_load_dwordx4 v22, s[68:71], s93 offen offset:1024 sc1 lds\n"
    "v_mfma_f32_16x16x32_bf16 v[104:107], a[200:203], a[152:155], v[104:107]\n"
    "v_exp_f32_e32 v95, v95\n"
    "v_mfma_f32_16x16x32_bf16 v[104:107], a[204:207], a[156:159], v[104:107]\n"
    "ds_read_b128 a[232:235], v202 offset:32768\n"
    "v_cvt_pk_bf16_f32 v91, v94, v95\n"
    "v_mfma_f32_16x16x32_bf16 v[108:111], a[208:211], a[144:147], v[48:51]\n"
    "v_exp_f32_e32 v103, v103\n"
    "v_mfma_f32_16x16x32_bf16 v[108:111], a[212:215], a[148:151], v[108:111]\n"
    "ds_read_b128 a[236:239], v203 offset:32768\n"
    "v_fma_f32 v144, v144, v138, v139\n"
    "v_mfma_f32_16x16x32_bf16 v[108:111], a[216:219], a[152:155], v[108:111]\n"
    "v_exp_f32_e32 v102, v102\n"
    "v_mfma_f32_16x16x32_bf16 v[108:111], a[220:223], a[156:159], v[108:111]\n"
    "ds_read_b128 a[240:243], v204 offset:32768\n"
    "v_cvt_pk_bf16_f32 v103, v102, v103\n"
    "v_mfma_f32_16x16x32_bf16 v[112:115], a[192:195], a[160:163], v[52:55]\n"
    "buffer_load_dwordx4 v22, s[68:71], s93 offen offset:2048 sc1 lds\n"
    "v_mfma_f32_16x16x32_bf16 v[112:115], a[196:199], a[164:167], v[112:115]\n"
    "v_exp_f32_e32 v101, v101\n"
    "v_mfma_f32_16x16x32_bf16 v[112:115], a[200:203], a[168:171], v[112:115]\n"
    "buffer_load_dwordx4 v22, s[68:71], s93 offen offset:3072 sc1 lds\n"
    "v_mfma_f32_16x16x32_bf16 v[112:115], a[204:207], a[172:175], v[112:115]\n"
    "v_exp_f32_e32 v100, v100\n"
    "v_mfma_f32_16x16x32_bf16 v[116:119], a[208:211], a[160:163], v[52:55]\n"
    "ds_read_b128 a[244:247], v205 offset:32768\n"
    "v_cvt_pk_bf16_f32 v102, v100, v101\n"
    "v_mfma_f32_16x16x32_bf16 v[116:119], a[212:215], a[164:167], v[116:119]\n"
    "v_exp_f32_e32 v99, v99\n"
    "v_mfma_f32_16x16x32_bf16 v[116:119], a[216:219], a[168:171], v[116:119]\n"
    "ds_read_b128 a[248:251], v206 offset:32768\n"
    "v_fma_f32 v145, v145, v138, v139\n"
    "v_mfma_f32_16x16x32_bf16 v[116:119], a[220:223], a[172:175], v[116:119]\n"
    "s_add_u32 m0, s92, 0x0\n"
    "v_exp_f32_e32 v98, v98\n"
    "v_mfma_f32_16x16x32_bf16 v[120:123], a[192:195], a[176:179], v[56:59]\n"
    "ds_read_b128 a[252:255], v207 offset:32768\n"
    "v_cvt_pk_bf16_f32 v101, v98, v99\n"
    "v_mfma_f32_16x16x32_bf16 v[120:123], a[196:199], a[180:183], v[120:123]\n"
    "v_exp_f32_e32 v97, v97\n"
    "v_mfma_f32_16x16x32_bf16 v[120:123], a[200:203], a[184:187], v[120:123]\n"
    "buffer_load_dwordx4 v21, s[68:71], s93 offen offset:0 sc1 lds\n"
    "v_mfma_f32_16x16x32_bf16 v[120:123], a[204:207], a[188:191], v[120:123]\n"
    "v_exp_f32_e32 v96, v96\n"
    "v_mfma_f32_16x16x32_bf16 v[124:127], a[208:211], a[176:179], v[56:59]\n"
    "v_cvt_pk_bf16_f32 v100, v96, v97\n"
    "v_fma_f32 v146, v146, v138, v139\n"
    "v_mfma_f32_16x16x32_bf16 v[124:127], a[212:215], a[180:183], v[124:127]\n"
    "v_exp_f32_e32 v104, v104\n"
    "v_mfma_f32_16x16x32_bf16 v[124:127], a[216:219], a[184:187], v[124:127]\n"
    "buffer_load_dwordx4 v21, s[68:71], s93 offen offset:1024 sc1 lds\n"
    "v_mfma_f32_16x16x32_bf16 v[124:127], a[220:223], a[188:191], v[124:127]\n"
    "s_waitcnt lgkmcnt(0)\n"
    "v_exp_f32_e32 v105, v105\n"
    "v_mfma_f32_16x16x32_bf16 a[0:3], v[68:71], a[224:227], a[0:3]\n"
    "v_cvt_pk_bf16_f32 v104, v104, v105\n"
    "v_fma_f32 v147, v147, v138, v139\n"
    "v_mfma_f32_16x16x32_bf16 a[4:7], v[68:71], a[228:231], a[4:7]\n"
    "v_exp_f32_e32 v106, v106\n"
    "v_mfma_f32_16x16x32_bf16 a[8:11], v[68:71], a[232:235], a[8:11]\n"
    "buffer_load_dwordx4 v21, s[68:71], s93 offen offset:2048 sc1 lds\n"
    "v_mfma_f32_16x16x32_bf16 a[12:15], v[68:71], a[236:239], a[12:15]\n"
    "v_exp_f32_e32 v107, v107\n"
    "v_mfma_f32_16x16x32_bf16 a[16:19], v[68:71], a[240:243], a[16:19]\n"
    "ds_read_b128 a[192:195], v16 offset:49152\n"
    "v_cvt_pk_bf16_f32 v105, v106, v107\n"
    "v_mfma_f32_16x16x32_bf16 a[20:23], v[68:71], a[244:247], a[20:23]\n"
    "v_exp_f32_e32 v108, v108\n"
    "v_mfma_f32_16x16x32_bf16 a[24:27], v[68:71], a[248:251], a[24:27]\n"
    "buffer_load_dwordx4 v21, s[68:71], s93 offen offset:3072 sc1 lds\n"
    "v_mfma_f32_16x16x32_bf16 a[28:31], v[68:71], a[252:255], a[28:31]\n"
    "s_add_u32 s93, s93, 0x8000\n"
    "v_exp_f32_e32 v109, v109\n"
    "v_mfma_f32_16x16x32_bf16 a[32:35], v[72:75], a[224:227], a[32:35]\n"
    "ds_read_b128 a[196:199], v17 offset:49152\n"
    "v_cvt_pk_bf16_f32 v106, v108, v109\n"
    "v_mfma_f32_16x16x32_bf16 a[36:39], v[72:75], a[228:231], a[36:39]\n"
    "v_exp_f32_e32 v110, v110\n"
    "v_mfma_f32_16x16x32_bf16 a[40:43], v[72:75], a[232:235], a[40:43]\n"
    "buffer_store_dwordx4 v[188:191], v132, s[68:71], s58 offen sc1\n"
    "v_mfma_f32_16x16x32_bf16 a[44:47], v[72:75], a[236:239], a[44:47]\n"
    "s_add_u32 s58, s58, 0x10000\n"
    "v_exp_f32_e32 v111, v111\n"
    "v_mfma_f32_16x16x32_bf16 a[48:51], v[72:75], a[240:243], a[48:51]\n"
    "ds_read_b128 a[200:203], v18 offset:49152\n"
    "v_cvt_pk_bf16_f32 v107, v110, v111\n"
    "v_mfma_f32_16x16x32_bf16 a[52:55], v[72:75], a[244:247], a[52:55]\n"
    "v_exp_f32_e32 v119, v119\n"
    "v_mfma_f32_16x16x32_bf16 a[56:59], v[72:75], a[248:251], a[56:59]\n"
    "buffer_load_dword v23, v61, s[88:91], 0 offen sc1\n"
    "v_mfma_f32_16x16x32_bf16 a[60:63], v[72:75], a[252:255], a[60:63]\n"
    "v_exp_f32_e32 v118, v118\n"
    "v_smfmac_f32_16x16x64_bf16 v[232:235], v[224:227], v[68:75], v228\n"
    "ds_read_b128 a[204:207], v19 offset:49152\n"
    "v_cvt_pk_bf16_f32 v119, v118, v119\n"
    "v_mfma_f32_16x16x32_bf16 a[64:67], v[84:87], a[224:227], a[64:67]\n"
    "ds_read_b128 a[208:211], v16 offset:53248\n"
    "v_lshlrev_b32_e32 v156, 16, v148\n"
    "v_mfma_f32_16x16x32_bf16 a[68:71], v[84:87], a[228:231], a[68:71]\n"
    "v_exp_f32_e32 v117, v117\n"
    "v_mfma_f32_16x16x32_bf16 a[72:75], v[84:87], a[232:235], a[72:75]\n"
    "ds_read_b128 a[212:215], v17 offset:53248\n"
    "v_and_b32_e32 v157, s95, v148\n"
    "v_mfma_f32_16x16x32_bf16 a[76:79], v[84:87], a[236:239], a[76:79]\n"
    "v_exp_f32_e32 v116, v116\n"
    "v_mfma_f32_16x16x32_bf16 a[80:83], v[84:87], a[240:243], a[80:83]\n"
    "ds_read_b128 a[216:219], v18 offset:53248\n"
    "v_cvt_pk_bf16_f32 v118, v116, v117\n"
    "v_mfma_f32_16x16x32_bf16 a[84:87], v[84:87], a[244:247], a[84:87]\n"
    "v_exp_f32_e32 v115, v115\n"
    "v_mfma_f32_16x16x32_bf16 a[88:91], v[84:87], a[248:251], a[88:91]\n"
    "ds_read_b128 a[220:223], v19 offset:53248\n"
    "v_lshl_add_u32 v158, v152, 16, v137\n"
    "v_mfma_f32_16x16x32_bf16 a[92:95], v[84:87], a[252:255], a[92:95]\n"
    "v_exp_f32_e32 v114, v114\n"
    "v_mfma_f32_16x16x32_bf16 a[96:99], v[88:91], a[224:227], a[96:99]\n"
    "v_cvt_pk_bf16_f32 v117, v114, v115\n"
    "v_bitop3_b32 v159, v152, s95, v137 bitop3:0x6a\n"
    "v_mfma_f32_16x16x32_bf16 a[100:103], v[88:91], a[228:231], a[100:103]\n"
    "v_exp_f32_e32 v113, v113\n"
    "v_mfma_f32_16x16x32_bf16 a[104:107], v[88:91], a[232:235], a[104:107]\n"
    "buffer_load_dwordx4 v[172:175], v130, s[76:79], s43 offen\n"
    "v_mfma_f32_16x16x32_bf16 a[108:111], v[88:91], a[236:239], a[108:111]\n"
    "v_exp_f32_e32 v112, v112\n"
    "v_mfma_f32_16x16x32_bf16 a[112:115], v[88:91], a[240:243], a[112:115]\n"
    "v_cvt_pk_bf16_f32 v116, v112, v113\n"
    "v_mul_f32_e32 v156, v140, v156\n"
    "v_mfma_f32_16x16x32_bf16 a[116:119], v[88:91], a[244:247], a[116:119]\n"
    "v_exp_f32_e32 v120, v120\n"
    "v_mfma_f32_16x16x32_bf16 a[120:123], v[88:91], a[248:251], a[120:123]\n"
    "buffer_load_dwordx4 v[176:179], v130, s[76:79], s43 offen offset:128\n"
    "v_mfma_f32_16x16x32_bf16 a[124:127], v[88:91], a[252:255], a[124:127]\n"
    "s_add_u32 s43, s43, 0x2000\n"
    "v_exp_f32_e32 v121, v121\n"
    "v_smfmac_f32_16x16x64_bf16 v[236:239], v[224:227], v[84:91], v228\n"
    "s_waitcnt lgkmcnt(0)\n"
    "v_cvt_pk_bf16_f32 v120, v120, v121\n"
    "v_mul_f32_e32 v157, v141, v157\n"
    "v_mfma_f32_16x16x32_bf16 v[64:67], a[192:195], a[128:131], v[44:47]\n"
    "v_exp_f32_e32 v122, v122\n"
    "v_mfma_f32_16x16x32_bf16 v[64:67], a[196:199], a[132:135], v[64:67]\n"
    "ds_read_b128 a[224:227], v200 offset:40960\n"
    "v_fmac_f32_dpp v156, v140, v158 quad_perm:[1,0,3,2] row_mask:0xf bank_mask:0xf\n"
    "v_mfma_f32_16x16x32_bf16 v[64:67], a[200:203], a[136:139], v[64:67]\n"
    "v_exp_f32_e32 v123, v123\n"
    "v_mfma_f32_16x16x32_bf16 v[64:67], a[204:207], a[140:143], v[64:67]\n"
    "ds_read_b128 a[228:231], v201 offset:40960\n"
    "v_cvt_pk_bf16_f32 v121, v122, v123\n"
    "v_mfma_f32_16x16x32_bf16 v[68:71], a[208:211], a[128:131], v[44:47]\n"
    "v_exp_f32_e32 v124, v124\n"
    "v_mfma_f32_16x16x32_bf16 v[68:71], a[212:215], a[132:135], v[68:71]\n"
    "ds_read_b128 a[232:235], v202 offset:40960\n"
    "v_fmac_f32_dpp v157, v141, v159 quad_perm:[1,0,3,2] row_mask:0xf bank_mask:0xf\n"
    "v_mfma_f32_16x16x32_bf16 v[68:71], a[216:219], a[136:139], v[68:71]\n"
    "v_exp_f32_e32 v125, v125\n"
    "v_mfma_f32_16x16x32_bf16 v[68:71], a[220:223], a[140:143], v[68:71]\n"
    "ds_read_b128 a[236:239], v203 offset:40960\n"
    "v_cvt_pk_bf16_f32 v122, v124, v125\n"
    "v_cvt_pk_bf16_f32 v160, v156, v157\n"
    "v_mfma_f32_16x16x32_bf16 v[72:75], a[192:195], a[144:147], v[48:51]\n"
    "v_exp_f32_e32 v126, v126\n"
    "v_mfma_f32_16x16x32_bf16 v[72:75], a[196:199], a[148:151], v[72:75]\n"
    "ds_read_b128 a[240:243], v204 offset:40960\n"
    "v_lshlrev_b32_e32 v156, 16, v149\n"
    "v_mfma_f32_16x16x32_bf16 v[72:75], a[200:203], a[152:155], v[72:75]\n"
    "v_exp_f32_e32 v127, v127\n"
    "v_mfma_f32_16x16x32_bf16 v[72:75], a[204:207], a[156:159], v[72:75]\n"
    "ds_read_b128 a[244:247], v205 offset:40960\n"
    "v_cvt_pk_bf16_f32 v123, v126, v127\n"
    "v_mfma_f32_16x16x32_bf16 v[76:79], a[208:211], a[144:147], v[48:51]\n"
    "v_exp_f32_e32 v71, v71\n"
    "v_mfma_f32_16x16x32_bf16 v[76:79], a[212:215], a[148:151], v[76:79]\n"
    "ds_read_b128 a[248:251], v206 offset:40960\n"
    "v_and_b32_e32 v157, s95, v149\n"
    "v_mfma_f32_16x16x32_bf16 v[76:79], a[216:219], a[152:155], v[76:79]\n"
    "v_exp_f32_e32 v70, v70\n"
    "v_mfma_f32_16x16x32_bf16 v[76:79], a[220:223], a[156:159], v[76:79]\n"
    "ds_read_b128 a[252:255], v207 offset:40960\n"
    "v_cvt_pk_bf16_f32 v71, v70, v71\n"
    "v_mfma_f32_16x16x32_bf16 v[80:83], a[192:195], a[160:163], v[52:55]\n"
    "v_lshl_add_u32 v158, v153, 16, v137\n"
    "v_bitop3_b32 v159, v153, s95, v137 bitop3:0x6a\n"
    "v_mfma_f32_16x16x32_bf16 v[80:83], a[196:199], a[164:167], v[80:83]\n"
    "v_exp_f32_e32 v69, v69\n"
    "v_mfma_f32_16x16x32_bf16 v[80:83], a[200:203], a[168:171], v[80:83]\n"
    "v_mul_f32_e32 v156, v142, v156\n"
    "v_mul_f32_e32 v157, v143, v157\n"
    "v_mfma_f32_16x16x32_bf16 v[80:83], a[204:207], a[172:175], v[80:83]\n"
    "v_exp_f32_e32 v68, v68\n"
    "v_mfma_f32_16x16x32_bf16 v[84:87], a[208:211], a[160:163], v[52:55]\n"
    "v_cvt_pk_bf16_f32 v70, v68, v69\n"
    "v_fmac_f32_dpp v156, v142, v158 quad_perm:[1,0,3,2] row_mask:0xf bank_mask:0xf\n"
    "v_mfma_f32_16x16x32_bf16 v[84:87], a[212:215], a[164:167], v[84:87]\n"
    "v_exp_f32_e32 v67, v67\n"
    "v_mfma_f32_16x16x32_bf16 v[84:87], a[216:219], a[168:171], v[84:87]\n"
    "v_fmac_f32_dpp v157, v143, v159 quad_perm:[1,0,3,2] row_mask:0xf bank_mask:0xf\n"
    "v_cvt_pk_bf16_f32 v161, v156, v157\n"
    "v_mfma_f32_16x16x32_bf16 v[84:87], a[220:223], a[172:175], v[84:87]\n"
    "v_exp_f32_e32 v66, v66\n"
    "v_mfma_f32_16x16x32_bf16 v[88:91], a[192:195], a[176:179], v[56:59]\n"
    "v_cvt_pk_bf16_f32 v69, v66, v67\n"
    "v_lshlrev_b32_e32 v156, 16, v150\n"
    "v_mfma_f32_16x16x32_bf16 v[88:91], a[196:199], a[180:183], v[88:91]\n"
    "v_exp_f32_e32 v65, v65\n"
    "v_mfma_f32_16x16x32_bf16 v[88:91], a[200:203], a[184:187], v[88:91]\n"
    "v_and_b32_e32 v157, s95, v150\n"
    "v_lshl_add_u32 v158, v154, 16, v137\n"
    "v_mfma_f32_16x16x32_bf16 v[88:91], a[204:207], a[188:191], v[88:91]\n"
    "v_exp_f32_e32 v64, v64\n"
    "v_mfma_f32_16x16x32_bf16 v[92:95], a[208:211], a[176:179], v[56:59]\n"
    "v_cvt_pk_bf16_f32 v68, v64, v65\n"
    "v_bitop3_b32 v159, v154, s95, v137 bitop3:0x6a\n"
    "v_mfma_f32_16x16x32_bf16 v[92:95], a[212:215], a[180:183], v[92:95]\n"
    "v_exp_f32_e32 v72, v72\n"
    "v_mfma_f32_16x16x32_bf16 v[92:95], a[216:219], a[184:187], v[92:95]\n"
    "v_mul_f32_e32 v156, v144, v156\n"
    "v_mul_f32_e32 v157, v145, v157\n"
    "v_mfma_f32_16x16x32_bf16 v[92:95], a[220:223], a[188:191], v[92:95]\n"
    "s_waitcnt lgkmcnt(0)\n"
    "v_exp_f32_e32 v73, v73\n"
    "v_mfma_f32_16x16x32_bf16 a[0:3], v[100:103], a[224:227], a[0:3]\n"
    "v_cvt_pk_bf16_f32 v72, v72, v73\n"
    "v_fmac_f32_dpp v156, v144, v158 quad_perm:[1,0,3,2] row_mask:0xf bank_mask:0xf\n"
    "v_mfma_f32_16x16x32_bf16 a[4:7], v[100:103], a[228:231], a[4:7]\n"
    "v_exp_f32_e32 v74, v74\n"
    "v_mfma_f32_16x16x32_bf16 a[8:11], v[100:103], a[232:235], a[8:11]\n"
    "ds_read_b128 a[192:195], v16 offset:57344\n"
    "v_fmac_f32_dpp v157, v145, v159 quad_perm:[1,0,3,2] row_mask:0xf bank_mask:0xf\n"
    "v_mfma_f32_16x16x32_bf16 a[12:15], v[100:103], a[236:239], a[12:15]\n"
    "v_exp_f32_e32 v75, v75\n"
    "v_mfma_f32_16x16x32_bf16 a[16:19], v[100:103], a[240:243], a[16:19]\n"
    "ds_read_b128 a[196:199], v17 offset:57344\n"
    "v_cvt_pk_bf16_f32 v73, v74, v75\n"
    "v_mfma_f32_16x16x32_bf16 a[20:23], v[100:103], a[244:247], a[20:23]\n"
    "v_exp_f32_e32 v76, v76\n"
    "v_mfma_f32_16x16x32_bf16 a[24:27], v[100:103], a[248:251], a[24:27]\n"
    "ds_read_b128 a[200:203], v18 offset:57344\n"
    "v_cvt_pk_bf16_f32 v162, v156, v157\n"
    "v_mfma_f32_16x16x32_bf16 a[28:31], v[100:103], a[252:255], a[28:31]\n"
    "v_exp_f32_e32 v77, v77\n"
    "v_mfma_f32_16x16x32_bf16 a[32:35], v[104:107], a[224:227], a[32:35]\n"
    "ds_read_b128 a[204:207], v19 offset:57344\n"
    "v_cvt_pk_bf16_f32 v74, v76, v77\n"
    "v_mfma_f32_16x16x32_bf16 a[36:39], v[104:107], a[228:231], a[36:39]\n"
    "v_exp_f32_e32 v78, v78\n"
    "v_mfma_f32_16x16x32_bf16 a[40:43], v[104:107], a[232:235], a[40:43]\n"
    "ds_read_b128 a[208:211], v16 offset:61440\n"
    "v_lshlrev_b32_e32 v156, 16, v151\n"
    "v_mfma_f32_16x16x32_bf16 a[44:47], v[104:107], a[236:239], a[44:47]\n"
    "v_exp_f32_e32 v79, v79\n"
    "v_mfma_f32_16x16x32_bf16 a[48:51], v[104:107], a[240:243], a[48:51]\n"
    "ds_read_b128 a[212:215], v17 offset:61440\n"
    "v_cvt_pk_bf16_f32 v75, v78, v79\n"
    "v_mfma_f32_16x16x32_bf16 a[52:55], v[104:107], a[244:247], a[52:55]\n"
    "v_exp_f32_e32 v87, v87\n"
    "v_mfma_f32_16x16x32_bf16 a[56:59], v[104:107], a[248:251], a[56:59]\n"
    "s_waitcnt vmcnt(2)\n"
    "ds_read_b128 a[216:219], v18 offset:61440\n"
    "v_max_u32_e32 v23, s96, v23\n"
    "v_mfma_f32_16x16x32_bf16 a[60:63], v[104:107], a[252:255], a[60:63]\n"
    "v_exp_f32_e32 v86, v86\n"
    "v_smfmac_f32_16x16x64_bf16 v[232:235], v[224:227], v[100:107], v228\n"
    "ds_write_b32 v229, v23\n"
    "v_cvt_pk_bf16_f32 v87, v86, v87\n"
    "v_mfma_f32_16x16x32_bf16 a[64:67], v[116:119], a[224:227], a[64:67]\n"
    "ds_read_b128 a[220:223], v19 offset:61440\n"
    "v_and_b32_e32 v157, s95, v151\n"
    "v_mfma_f32_16x16x32_bf16 a[68:71], v[116:119], a[228:231], a[68:71]\n"
    "v_exp_f32_e32 v85, v85\n"
    "v_mfma_f32_16x16x32_bf16 a[72:75], v[116:119], a[232:235], a[72:75]\n"
    "ds_read_b128 v[180:183], v134\n"
    "v_lshl_add_u32 v158, v155, 16, v137\n"
    "v_mfma_f32_16x16x32_bf16 a[76:79], v[116:119], a[236:239], a[76:79]\n"
    "v_exp_f32_e32 v84, v84\n"
    "v_mfma_f32_16x16x32_bf16 a[80:83], v[116:119], a[240:243], a[80:83]\n"
    "v_cvt_pk_bf16_f32 v86, v84, v85\n"
    "v_add_u32_e32 v134, 0x200, v134\n"
    "v_mfma_f32_16x16x32_bf16 a[84:87], v[116:119], a[244:247], a[84:87]\n"
    "v_exp_f32_e32 v83, v83\n"
    "v_mfma_f32_16x16x32_bf16 a[88:91], v[116:119], a[248:251], a[88:91]\n"
    "ds_read_b32 v231, v230\n"
    "v_mov_b32_e32 v62, s60\n"
    "v_mfma_f32_16x16x32_bf16 a[92:95], v[116:119], a[252:255], a[92:95]\n"
    "v_exp_f32_e32 v82, v82\n"
    "v_mfma_f32_16x16x32_bf16 a[96:99], v[120:123], a[224:227], a[96:99]\n"
    "v_cvt_pk_bf16_f32 v85, v82, v83\n"
    "v_bitop3_b32 v159, v155, s95, v137 bitop3:0x6a\n"
    "v_mfma_f32_16x16x32_bf16 a[100:103], v[120:123], a[228:231], a[100:103]\n"
    "v_exp_f32_e32 v81, v81\n"
    "v_mfma_f32_16x16x32_bf16 a[104:107], v[120:123], a[232:235], a[104:107]\n"
    "v_mul_f32_e32 v156, v146, v156\n"
    "v_mul_f32_e32 v157, v147, v157\n"
    "v_mfma_f32_16x16x32_bf16 a[108:111], v[120:123], a[236:239], a[108:111]\n"
    "v_exp_f32_e32 v80, v80\n"
    "v_mfma_f32_16x16x32_bf16 a[112:115], v[120:123], a[240:243], a[112:115]\n"
    "v_cvt_pk_bf16_f32 v84, v80, v81\n"
    "v_fmac_f32_dpp v156, v146, v158 quad_perm:[1,0,3,2] row_mask:0xf bank_mask:0xf\n"
    "v_mfma_f32_16x16x32_bf16 a[116:119], v[120:123], a[244:247], a[116:119]\n"
    "v_exp_f32_e32 v88, v88\n"
    "v_mfma_f32_16x16x32_bf16 a[120:123], v[120:123], a[248:251], a[120:123]\n"
    "s_waitcnt lgkmcnt(0)\n"
    "v_cmp_lt_u32_e32 vcc, s96, v231\n"
    "v_fmac_f32_dpp v157, v147, v159 quad_perm:[1,0,3,2] row_mask:0xf bank_mask:0xf\n"
    "v_mfma_f32_16x16x32_bf16 a[124:127], v[120:123], a[252:255], a[124:127]\n"
    "v_exp_f32_e32 v89, v89\n"
    "v_smfmac_f32_16x16x64_bf16 v[236:239], v[224:227], v[116:123], v228\n"
    "s_cbranch_vccnz .Lspin_l2\n"
    ".Lback_l2:\n"
    "v_cvt_pk_bf16_f32 v88, v88, v89\n"
    "v_cvt_pk_bf16_f32 v163, v156, v157\n"
    "v_mfma_f32_16x16x32_bf16 v[96:99], a[192:195], a[128:131], v[44:47]\n"
    "v_exp_f32_e32 v90, v90\n"
    "v_mfma_f32_16x16x32_bf16 v[96:99], a[196:199], a[132:135], v[96:99]\n"
    "buffer_store_dword v62, v60, s[88:91], 0 offen sc1\n"
    "v_mfma_f32_16x16x32_bf16 v[96:99], a[200:203], a[136:139], v[96:99]\n"
    "s_add_u32 m0, s92, 0x10000\n"
    "v_exp_f32_e32 v91, v91\n"
    "v_mfma_f32_16x16x32_bf16 v[96:99], a[204:207], a[140:143], v[96:99]\n"
    "s_sub_u32 s96, s96, 1\n"
    "ds_read_b128 a[224:227], v200 offset:49152\n"
    "v_cvt_pk_bf16_f32 v89, v90, v91\n"
    "v_mfma_f32_16x16x32_bf16 v[100:103], a[208:211], a[128:131], v[44:47]\n"
    "s_sub_u32 s60, s60, 1\n"
    "v_exp_f32_e32 v92, v92\n"
    "v_mfma_f32_16x16x32_bf16 v[100:103], a[212:215], a[132:135], v[100:103]\n"
    "buffer_load_dwordx4 v22, s[68:71], s93 offen offset:0 sc1 lds\n"
    "v_mfma_f32_16x16x32_bf16 v[100:103], a[216:219], a[136:139], v[100:103]\n"
    "v_exp_f32_e32 v93, v93\n"
    "v_mfma_f32_16x16x32_bf16 v[100:103], a[220:223], a[140:143], v[100:103]\n"
    "ds_read_b128 a[228:231], v201 offset:49152\n"
    "v_cvt_pk_bf16_f32 v90, v92, v93\n"
    "v_mfma_f32_16x16x32_bf16 v[104:107], a[192:195], a[144:147], v[48:51]\n"
    "v_exp_f32_e32 v94, v94\n"
    "v_mfma_f32_16x16x32_bf16 v[104:107], a[196:199], a[148:151], v[104:107]\n"
    "buffer_load_dwordx4 v22, s[68:71], s93 offen offset:1024 sc1 lds\n"
    "v_mfma_f32_16x16x32_bf16 v[104:107], a[200:203], a[152:155], v[104:107]\n"
    "v_exp_f32_e32 v95, v95\n"
    "v_mfma_f32_16x16x32_bf16 v[104:107], a[204:207], a[156:159], v[104:107]\n"
    "ds_read_b128 a[232:235], v202 offset:49152\n"
    "v_cvt_pk_bf16_f32 v91, v94, v95\n"
    "v_mfma_f32_16x16x32_bf16 v[108:111], a[208:211], a[144:147], v[48:51]\n"
    "v_exp_f32_e32 v103, v103\n"
    "v_mfma_f32_16x16x32_bf16 v[108:111], a[212:215], a[148:151], v[108:111]\n"
    "s_waitcnt vmcnt(31)\n"
    "ds_read_b128 a[236:239], v203 offset:49152\n"
    "v_cvt_f32_i32_e32 v164, v164\n"
    "v_mfma_f32_16x16x32_bf16 v[108:111], a[216:219], a[152:155], v[108:111]\n"
    "v_exp_f32_e32 v102, v102\n"
    "v_mfma_f32_16x16x32_bf16 v[108:111], a[220:223], a[156:159], v[108:111]\n"
    "ds_read_b128 a[240:243], v204 offset:49152\n"
    "v_cvt_pk_bf16_f32 v103, v102, v103\n"
    "v_mfma_f32_16x16x32_bf16 v[112:115], a[192:195], a[160:163], v[52:55]\n"
    "buffer_load_dwordx4 v22, s[68:71], s93 offen offset:2048 sc1 lds\n"
    "v_mfma_f32_16x16x32_bf16 v[112:115], a[196:199], a[164:167], v[112:115]\n"
    "v_exp_f32_e32 v101, v101\n"
    "v_mfma_f32_16x16x32_bf16 v[112:115], a[200:203], a[168:171], v[112:115]\n"
    "buffer_load_dwordx4 v22, s[68:71], s93 offen offset:3072 sc1 lds\n"
    "v_mfma_f32_16x16x32_bf16 v[112:115], a[204:207], a[172:175], v[112:115]\n"
    "v_exp_f32_e32 v100, v100\n"
    "v_mfma_f32_16x16x32_bf16 v[116:119], a[208:211], a[160:163], v[52:55]\n"
    "ds_read_b128 a[244:247], v205 offset:49152\n"
    "v_cvt_pk_bf16_f32 v102, v100, v101\n"
    "v_mfma_f32_16x16x32_bf16 v[116:119], a[212:215], a[164:167], v[116:119]\n"
    "v_exp_f32_e32 v99, v99\n"
    "v_mfma_f32_16x16x32_bf16 v[116:119], a[216:219], a[168:171], v[116:119]\n"
    "ds_read_b128 a[248:251], v206 offset:49152\n"
    "v_cvt_f32_i32_e32 v165, v165\n"
    "v_mfma_f32_16x16x32_bf16 v[116:119], a[220:223], a[172:175], v[116:119]\n"
    "s_add_u32 m0, s92, 0x4000\n"
    "v_exp_f32_e32 v98, v98\n"
    "v_mfma_f32_16x16x32_bf16 v[120:123], a[192:195], a[176:179], v[56:59]\n"
    "ds_read_b128 a[252:255], v207 offset:49152\n"
    "v_cvt_pk_bf16_f32 v101, v98, v99\n"
    "v_mfma_f32_16x16x32_bf16 v[120:123], a[196:199], a[180:183], v[120:123]\n"
    "v_exp_f32_e32 v97, v97\n"
    "v_mfma_f32_16x16x32_bf16 v[120:123], a[200:203], a[184:187], v[120:123]\n"
    "buffer_load_dwordx4 v21, s[68:71], s93 offen offset:0 sc1 lds\n"
    "v_mfma_f32_16x16x32_bf16 v[120:123], a[204:207], a[188:191], v[120:123]\n"
    "v_exp_f32_e32 v96, v96\n"
    "v_mfma_f32_16x16x32_bf16 v[124:127], a[208:211], a[176:179], v[56:59]\n"
    "v_cvt_pk_bf16_f32 v100, v96, v97\n"
    "v_cvt_f32_i32_e32 v166, v166\n"
    "v_mfma_f32_16x16x32_bf16 v[124:127], a[212:215], a[180:183], v[124:127]\n"
    "v_exp_f32_e32 v104, v104\n"
    "v_mfma_f32_16x16x32_bf16 v[124:127], a[216:219], a[184:187], v[124:127]\n"
    "buffer_load_dwordx4 v21, s[68:71], s93 offen offset:1024 sc1 lds\n"
    "v_mfma_f32_16x16x32_bf16 v[124:127], a[220:223], a[188:191], v[124:127]\n"
    "s_waitcnt lgkmcnt(0)\n"
    "v_exp_f32_e32 v105, v105\n"
    "v_mfma_f32_16x16x32_bf16 a[0:3], v[68:71], a[224:227], a[0:3]\n"
    "v_cvt_pk_bf16_f32 v104, v104, v105\n"
    "v_cvt_f32_i32_e32 v167, v167\n"
    "v_mfma_f32_16x16x32_bf16 a[4:7], v[68:71], a[228:231], a[4:7]\n"
    "v_exp_f32_e32 v106, v106\n"
    "v_mfma_f32_16x16x32_bf16 a[8:11], v[68:71], a[232:235], a[8:11]\n"
    "buffer_load_dwordx4 v21, s[68:71], s93 offen offset:2048 sc1 lds\n"
    "v_mfma_f32_16x16x32_bf16 a[12:15], v[68:71], a[236:239], a[12:15]\n"
    "v_exp_f32_e32 v107, v107\n"
    "v_mfma_f32_16x16x32_bf16 a[16:19], v[68:71], a[240:243], a[16:19]\n"
    "ds_read_b128 a[192:195], v16 offset:0\n"
    "v_cvt_pk_bf16_f32 v105, v106, v107\n"
    "v_mfma_f32_16x16x32_bf16 a[20:23], v[68:71], a[244:247], a[20:23]\n"
    "v_exp_f32_e32 v108, v108\n"
    "v_mfma_f32_16x16x32_bf16 a[24:27], v[68:71], a[248:251], a[24:27]\n"
    "buffer_load_dwordx4 v21, s[68:71], s93 offen offset:3072 sc1 lds\n"
    "v_mfma_f32_16x16x32_bf16 a[28:31], v[68:71], a[252:255], a[28:31]\n"
    "s_add_u32 s93, s93, 0x8000\n"
    "v_exp_f32_e32 v109, v109\n"
    "v_mfma_f32_16x16x32_bf16 a[32:35], v[72:75], a[224:227], a[32:35]\n"
    "ds_read_b128 a[196:199], v17 offset:0\n"
    "v_cvt_pk_bf16_f32 v106, v108, v109\n"
    "v_mfma_f32_16x16x32_bf16 a[36:39], v[72:75], a[228:231], a[36:39]\n"
    "v_exp_f32_e32 v110, v110\n"
    "v_mfma_f32_16x16x32_bf16 a[40:43], v[72:75], a[232:235], a[40:43]\n"
    "buffer_store_dwordx4 v[160:163], v131, s[68:71], s49 offen sc1\n"
    "v_mfma_f32_16x16x32_bf16 a[44:47], v[72:75], a[236:239], a[44:47]\n"
    "s_add_u32 s49, s49, 0x10000\n"
    "v_exp_f32_e32 v111, v111\n"
    "v_mfma_f32_16x16x32_bf16 a[48:51], v[72:75], a[240:243], a[48:51]\n"
    "ds_read_b128 a[200:203], v18 offset:0\n"
    "v_cvt_pk_bf16_f32 v107, v110, v111\n"
    "v_mfma_f32_16x16x32_bf16 a[52:55], v[72:75], a[244:247], a[52:55]\n"
    "v_exp_f32_e32 v119, v119\n"
    "v_mfma_f32_16x16x32_bf16 a[56:59], v[72:75], a[248:251], a[56:59]\n"
    "buffer_load_dword v23, v61, s[88:91], 0 offen sc1\n"
    "v_mfma_f32_16x16x32_bf16 a[60:63], v[72:75], a[252:255], a[60:63]\n"
    "v_exp_f32_e32 v118, v118\n"
    "v_smfmac_f32_16x16x64_bf16 v[232:235], v[224:227], v[68:75], v228\n"
    "ds_read_b128 a[204:207], v19 offset:0\n"
    "v_cvt_pk_bf16_f32 v119, v118, v119\n"
    "v_mfma_f32_16x16x32_bf16 a[64:67], v[84:87], a[224:227], a[64:67]\n"
    "ds_read_b128 a[208:211], v16 offset:4096\n"
    "v_cvt_f32_i32_e32 v168, v168\n"
    "v_mfma_f32_16x16x32_bf16 a[68:71], v[84:87], a[228:231], a[68:71]\n"
    "v_exp_f32_e32 v117, v117\n"
    "v_mfma_f32_16x16x32_bf16 a[72:75], v[84:87], a[232:235], a[72:75]\n"
    "ds_read_b128 a[212:215], v17 offset:4096\n"
    "v_cvt_f32_i32_e32 v169, v169\n"
    "v_mfma_f32_16x16x32_bf16 a[76:79], v[84:87], a[236:239], a[76:79]\n"
    "v_exp_f32_e32 v116, v116\n"
    "v_mfma_f32_16x16x32_bf16 a[80:83], v[84:87], a[240:243], a[80:83]\n"
    "ds_read_b128 a[216:219], v18 offset:4096\n"
    "v_cvt_pk_bf16_f32 v118, v116, v117\n"
    "v_mfma_f32_16x16x32_bf16 a[84:87], v[84:87], a[244:247], a[84:87]\n"
    "v_exp_f32_e32 v115, v115\n"
    "v_mfma_f32_16x16x32_bf16 a[88:91], v[84:87], a[248:251], a[88:91]\n"
    "ds_read_b128 a[220:223], v19 offset:4096\n"
    "v_cvt_f32_i32_e32 v170, v170\n"
    "v_mfma_f32_16x16x32_bf16 a[92:95], v[84:87], a[252:255], a[92:95]\n"
    "v_exp_f32_e32 v114, v114\n"
    "v_mfma_f32_16x16x32_bf16 a[96:99], v[88:91], a[224:227], a[96:99]\n"
    "v_cvt_pk_bf16_f32 v117, v114, v115\n"
    "v_cvt_f32_i32_e32 v171, v171\n"
    "v_mfma_f32_16x16x32_bf16 a[100:103], v[88:91], a[228:231], a[100:103]\n"
    "v_exp_f32_e32 v113, v113\n"
    "v_mfma_f32_16x16x32_bf16 a[104:107], v[88:91], a[232:235], a[104:107]\n"
    "buffer_load_dwordx4 v[140:143], v128, s[72:75], s42 offen\n"
    "v_mfma_f32_16x16x32_bf16 a[108:111], v[88:91], a[236:239], a[108:111]\n"
    "v_exp_f32_e32 v112, v112\n"
    "v_mfma_f32_16x16x32_bf16 a[112:115], v[88:91], a[240:243], a[112:115]\n"
    "v_cvt_pk_bf16_f32 v116, v112, v113\n"
    "v_fma_f32 v164, v164, v180, v181\n"
    "v_mfma_f32_16x16x32_bf16 a[116:119], v[88:91], a[244:247], a[116:119]\n"
    "v_exp_f32_e32 v120, v120\n"
    "v_mfma_f32_16x16x32_bf16 a[120:123], v[88:91], a[248:251], a[120:123]\n"
    "buffer_load_dwordx4 v[144:147], v128, s[72:75], s42 offen offset:16\n"
    "v_mfma_f32_16x16x32_bf16 a[124:127], v[88:91], a[252:255], a[124:127]\n"
    "s_add_u32 s42, s42, 0x2000\n"
    "v_exp_f32_e32 v121, v121\n"
    "v_smfmac_f32_16x16x64_bf16 v[236:239], v[224:227], v[84:91], v228\n"
    "s_waitcnt lgkmcnt(0)\n"
    "s_min_u32 s42, s42, 0x1e000\n"
    "v_cvt_pk_bf16_f32 v120, v120, v121\n"
    "v_fma_f32 v165, v165, v180, v181\n"
    "v_mfma_f32_16x16x32_bf16 v[64:67], a[192:195], a[128:131], v[44:47]\n"
    "v_exp_f32_e32 v122, v122\n"
    "v_mfma_f32_16x16x32_bf16 v[64:67], a[196:199], a[132:135], v[64:67]\n"
    "buffer_load_dwordx4 v[148:151], v129, s[80:83], s48 offen\n"
    "v_mfma_f32_16x16x32_bf16 v[64:67], a[200:203], a[136:139], v[64:67]\n"
    "v_exp_f32_e32 v123, v123\n"
    "v_mfma_f32_16x16x32_bf16 v[64:67], a[204:207], a[140:143], v[64:67]\n"
    "ds_read_b128 a[224:227], v200 offset:57344\n"
    "v_cvt_pk_bf16_f32 v121, v122, v123\n"
    "v_mfma_f32_16x16x32_bf16 v[68:71], a[208:211], a[128:131], v[44:47]\n"
    "v_exp_f32_e32 v124, v124\n"
    "v_mfma_f32_16x16x32_bf16 v[68:71], a[212:215], a[132:135], v[68:71]\n"
    "buffer_load_dwordx4 v[152:155], v129, s[84:87], s48 offen\n"
    "v_mfma_f32_16x16x32_bf16 v[68:71], a[216:219], a[136:139], v[68:71]\n"
    "s_add_u32 s48, s48, 0x1000\n"
    "v_exp_f32_e32 v125, v125\n"
    "v_mfma_f32_16x16x32_bf16 v[68:71], a[220:223], a[140:143], v[68:71]\n"
    "s_min_u32 s48, s48, 0xf000\n"
    "ds_read_b128 a[228:231], v201 offset:57344\n"
    "v_cvt_pk_bf16_f32 v122, v124, v125\n"
    "v_mfma_f32_16x16x32_bf16 v[72:75], a[192:195], a[144:147], v[48:51]\n"
    "v_exp_f32_e32 v126, v126\n"
    "v_mfma_f32_16x16x32_bf16 v[72:75], a[196:199], a[148:151], v[72:75]\n"
    "ds_read_b128 a[232:235], v202 offset:57344\n"
    "v_fma_f32 v166, v166, v180, v181\n"
    "v_mfma_f32_16x16x32_bf16 v[72:75], a[200:203], a[152:155], v[72:75]\n"
    "v_exp_f32_e32 v127, v127\n"
    "v_mfma_f32_16x16x32_bf16 v[72:75], a[204:207], a[156:159], v[72:75]\n"
    "ds_read_b128 a[236:239], v203 offset:57344\n"
    "v_cvt_pk_bf16_f32 v123, v126, v127\n"
    "v_mfma_f32_16x16x32_bf16 v[76:79], a[208:211], a[144:147], v[48:51]\n"
    "v_exp_f32_e32 v71, v71\n"
    "v_mfma_f32_16x16x32_bf16 v[76:79], a[212:215], a[148:151], v[76:79]\n"
    "ds_read_b128 a[240:243], v204 offset:57344\n"
    "v_fma_f32 v167, v167, v180, v181\n"
    "v_mfma_f32_16x16x32_bf16 v[76:79], a[216:219], a[152:155], v[76:79]\n"
    "v_exp_f32_e32 v70, v70\n"
    "v_mfma_f32_16x16x32_bf16 v[76:79], a[220:223], a[156:159], v[76:79]\n"
    "ds_read_b128 a[244:247], v205 offset:57344\n"
    "v_cvt_pk_bf16_f32 v71, v70, v71\n"
    "v_mfma_f32_16x16x32_bf16 v[80:83], a[192:195], a[160:163], v[52:55]\n"
    "ds_read_b128 a[248:251], v206 offset:57344\n"
    "v_fma_f32 v168, v168, v182, v183\n"
    "v_mfma_f32_16x16x32_bf16 v[80:83], a[196:199], a[164:167], v[80:83]\n"
    "v_exp_f32_e32 v69, v69\n"
    "v_mfma_f32_16x16x32_bf16 v[80:83], a[200:203], a[168:171], v[80:83]\n"
    "ds_read_b128 a[252:255], v207 offset:57344\n"
    "v_fma_f32 v169, v169, v182, v183\n"
    "v_mfma_f32_16x16x32_bf16 v[80:83], a[204:207], a[172:175], v[80:83]\n"
    "v_exp_f32_e32 v68, v68\n"
    "v_mfma_f32_16x16x32_bf16 v[84:87], a[208:211], a[160:163], v[52:55]\n"
    "ds_read_b64 v[138:139], v133\n"
    "v_cvt_pk_bf16_f32 v70, v68, v69\n"
    "v_mfma_f32_16x16x32_bf16 v[84:87], a[212:215], a[164:167], v[84:87]\n"
    "v_exp_f32_e32 v67, v67\n"
    "v_mfma_f32_16x16x32_bf16 v[84:87], a[216:219], a[168:171], v[84:87]\n"
    "v_add_u32_e32 v133, 0x200, v133\n"
    "v_fma_f32 v170, v170, v182, v183\n"
    "v_mfma_f32_16x16x32_bf16 v[84:87], a[220:223], a[172:175], v[84:87]\n"
    "v_exp_f32_e32 v66, v66\n"
    "v_mfma_f32_16x16x32_bf16 v[88:91], a[192:195], a[176:179], v[56:59]\n"
    "v_cvt_pk_bf16_f32 v69, v66, v67\n"
    "v_fma_f32 v171, v171, v182, v183\n"
    "v_mfma_f32_16x16x32_bf16 v[88:91], a[196:199], a[180:183], v[88:91]\n"
    "v_exp_f32_e32 v65, v65\n"
    "v_mfma_f32_16x16x32_bf16 v[88:91], a[200:203], a[184:187], v[88:91]\n"
    "v_cvt_pk_bf16_f32 v184, v164, v165\n"
    "v_cvt_pk_bf16_f32 v185, v166, v167\n"
    "v_mfma_f32_16x16x32_bf16 v[88:91], a[204:207], a[188:191], v[88:91]\n"
    "v_exp_f32_e32 v64, v64\n"
    "v_mfma_f32_16x16x32_bf16 v[92:95], a[208:211], a[176:179], v[56:59]\n"
    "v_cvt_pk_bf16_f32 v68, v64, v65\n"
    "v_cvt_pk_bf16_f32 v186, v168, v169\n"
    "v_mfma_f32_16x16x32_bf16 v[92:95], a[212:215], a[180:183], v[92:95]\n"
    "v_exp_f32_e32 v72, v72\n"
    "v_mfma_f32_16x16x32_bf16 v[92:95], a[216:219], a[184:187], v[92:95]\n"
    "v_cvt_pk_bf16_f32 v187, v170, v171\n"
    "v_mfma_f32_16x16x32_bf16 v[92:95], a[220:223], a[188:191], v[92:95]\n"
    "s_waitcnt lgkmcnt(0)\n"
    "v_exp_f32_e32 v73, v73\n"
    "v_mfma_f32_16x16x32_bf16 a[0:3], v[100:103], a[224:227], a[0:3]\n"
    "ds_write_b128 v135, v[184:187]\n"
    "v_cvt_pk_bf16_f32 v72, v72, v73\n"
    "v_mfma_f32_16x16x32_bf16 a[4:7], v[100:103], a[228:231], a[4:7]\n"
    "v_exp_f32_e32 v74, v74\n"
    "v_mfma_f32_16x16x32_bf16 a[8:11], v[100:103], a[232:235], a[8:11]\n"
    "s_waitcnt vmcnt(28)\n"
    "ds_read_b128 a[192:195], v16 offset:8192\n"
    "v_cvt_f32_i32_e32 v240, v240\n"
    "v_mfma_f32_16x16x32_bf16 a[12:15], v[100:103], a[236:239], a[12:15]\n"
    "v_exp_f32_e32 v75, v75\n"
    "v_mfma_f32_16x16x32_bf16 a[16:19], v[100:103], a[240:243], a[16:19]\n"
    "ds_read_b128 a[196:199], v17 offset:8192\n"
    "v_cvt_pk_bf16_f32 v73, v74, v75\n"
    "v_mfma_f32_16x16x32_bf16 a[20:23], v[100:103], a[244:247], a[20:23]\n"
    "v_exp_f32_e32 v76, v76\n"
    "v_mfma_f32_16x16x32_bf16 a[24:27], v[100:103], a[248:251], a[24:27]\n"
    "ds_read_b128 a[200:203], v18 offset:8192\n"
    "v_cvt_f32_i32_e32 v241, v241\n"
    "v_mfma_f32_16x16x32_bf16 a[28:31], v[100:103], a[252:255], a[28:31]\n"
    "v_exp_f32_e32 v77, v77\n"
    "v_mfma_f32_16x16x32_bf16 a[32:35], v[104:107], a[224:227], a[32:35]\n"
    "ds_read_b128 a[204:207], v19 offset:8192\n"
    "v_cvt_pk_bf16_f32 v74, v76, v77\n"
    "v_mfma_f32_16x16x32_bf16 a[36:39], v[104:107], a[228:231], a[36:39]\n"
    "v_exp_f32_e32 v78, v78\n"
    "v_mfma_f32_16x16x32_bf16 a[40:43], v[104:107], a[232:235], a[40:43]\n"
    "ds_read_b128 a[208:211], v16 offset:12288\n"
    "v_cvt_f32_i32_e32 v242, v242\n"
    "v_mfma_f32_16x16x32_bf16 a[44:47], v[104:107], a[236:239], a[44:47]\n"
    "v_exp_f32_e32 v79, v79\n"
    "v_mfma_f32_16x16x32_bf16 a[48:51], v[104:107], a[240:243], a[48:51]\n"
    "ds_read_b128 a[212:215], v17 offset:12288\n"
    "v_cvt_pk_bf16_f32 v75, v78, v79\n"
    "v_mfma_f32_16x16x32_bf16 a[52:55], v[104:107], a[244:247], a[52:55]\n"
    "v_exp_f32_e32 v87, v87\n"
    "v_mfma_f32_16x16x32_bf16 a[56:59], v[104:107], a[248:251], a[56:59]\n"
    "s_waitcnt vmcnt(4)\n"
    "ds_read_b128 a[216:219], v18 offset:12288\n"
    "v_max_u32_e32 v23, s96, v23\n"
    "v_mfma_f32_16x16x32_bf16 a[60:63], v[104:107], a[252:255], a[60:63]\n"
    "v_exp_f32_e32 v86, v86\n"
    "v_smfmac_f32_16x16x64_bf16 v[232:235], v[224:227], v[100:107], v228\n"
    "ds_write_b32 v229, v23\n"
    "v_cvt_pk_bf16_f32 v87, v86, v87\n"
    "v_mfma_f32_16x16x32_bf16 a[64:67], v[116:119], a[224:227], a[64:67]\n"
    "ds_read_b128 a[220:223], v19 offset:12288\n"
    "v_cvt_f32_i32_e32 v243, v243\n"
    "v_cvt_f32_i32_e32 v244, v244\n"
    "v_mfma_f32_16x16x32_bf16 a[68:71], v[116:119], a[228:231], a[68:71]\n"
    "v_exp_f32_e32 v85, v85\n"
    "v_mfma_f32_16x16x32_bf16 a[72:75], v[116:119], a[232:235], a[72:75]\n"
    "ds_read_b64_tr_b16 v[188:189], v136\n"
    "v_cvt_f32_i32_e32 v245, v245\n"
    "v_mfma_f32_16x16x32_bf16 a[76:79], v[116:119], a[236:239], a[76:79]\n"
    "v_exp_f32_e32 v84, v84\n"
    "v_mfma_f32_16x16x32_bf16 a[80:83], v[116:119], a[240:243], a[80:83]\n"
    "ds_read_b64_tr_b16 v[190:191], v136 offset:512\n"
    "v_cvt_pk_bf16_f32 v86, v84, v85\n"
    "v_mfma_f32_16x16x32_bf16 a[84:87], v[116:119], a[244:247], a[84:87]\n"
    "v_exp_f32_e32 v83, v83\n"
    "v_mfma_f32_16x16x32_bf16 a[88:91], v[116:119], a[248:251], a[88:91]\n"
    "ds_read_b32 v231, v230\n"
    "v_mov_b32_e32 v62, s60\n"
    "v_mfma_f32_16x16x32_bf16 a[92:95], v[116:119], a[252:255], a[92:95]\n"
    "v_exp_f32_e32 v82, v82\n"
    "v_mfma_f32_16x16x32_bf16 a[96:99], v[120:123], a[224:227], a[96:99]\n"
    "v_cvt_pk_bf16_f32 v85, v82, v83\n"
    "v_cvt_f32_i32_e32 v246, v246\n"
    "v_mfma_f32_16x16x32_bf16 a[100:103], v[120:123], a[228:231], a[100:103]\n"
    "v_exp_f32_e32 v81, v81\n"
    "v_mfma_f32_16x16x32_bf16 a[104:107], v[120:123], a[232:235], a[104:107]\n"
    "v_cvt_f32_i32_e32 v247, v247\n"
    "v_fma_f32 v240, v240, v138, v139\n"
    "v_mfma_f32_16x16x32_bf16 a[108:111], v[120:123], a[236:239], a[108:111]\n"
    "v_exp_f32_e32 v80, v80\n"
    "v_mfma_f32_16x16x32_bf16 a[112:115], v[120:123], a[240:243], a[112:115]\n"
    "v_cvt_pk_bf16_f32 v84, v80, v81\n"
    "v_fma_f32 v241, v241, v138, v139\n"
    "v_mfma_f32_16x16x32_bf16 a[116:119], v[120:123], a[244:247], a[116:119]\n"
    "v_exp_f32_e32 v88, v88\n"
    "v_mfma_f32_16x16x32_bf16 a[120:123], v[120:123], a[248:251], a[120:123]\n"
    "s_waitcnt lgkmcnt(0)\n"
    "v_cmp_lt_u32_e32 vcc, s96, v231\n"
    "v_fma_f32 v242, v242, v138, v139\n"
    "v_mfma_f32_16x16x32_bf16 a[124:127], v[120:123], a[252:255], a[124:127]\n"
    "v_exp_f32_e32 v89, v89\n"
    "v_smfmac_f32_16x16x64_bf16 v[236:239], v[224:227], v[116:123], v228\n"
    "s_cbranch_vccnz .Lspin_l3\n"
    ".Lback_l3:\n"
    "v_cvt_pk_bf16_f32 v88, v88, v89\n"
    "v_fma_f32 v243, v243, v138, v139\n"
    "s_sub_u32 s94, s94, 1\n"
    "s_cbranch_scc0 .Lattn_loop\n"
    ".Lattn_tail:\n"
    "v_mfma_f32_16x16x32_bf16 v[96:99], a[192:195], a[128:131], v[44:47]\n"
    "v_exp_f32_e32 v90, v90\n"
    "v_mfma_f32_16x16x32_bf16 v[96:99], a[196:199], a[132:135], v[96:99]\n"
    "buffer_store_dword v62, v60, s[88:91], 0 offen sc1\n"
    "v_mfma_f32_16x16x32_bf16 v[96:99], a[200:203], a[136:139], v[96:99]\n"
    "s_add_u32 m0, s92, 0x14000\n"
    "v_exp_f32_e32 v91, v91\n"
    "v_mfma_f32_16x16x32_bf16 v[96:99], a[204:207], a[140:143], v[96:99]\n"
    "s_sub_u32 s96, s96, 1\n"
    "ds_read_b128 a[224:227], v200 offset:0\n"
    "v_cvt_pk_bf16_f32 v89, v90, v91\n"
    "v_mfma_f32_16x16x32_bf16 v[100:103], a[208:211], a[128:131], v[44:47]\n"
    "s_sub_u32 s60, s60, 1\n"
    "v_exp_f32_e32 v92, v92\n"
    "v_mfma_f32_16x16x32_bf16 v[100:103], a[212:215], a[132:135], v[100:103]\n"
    "buffer_load_dwordx4 v22, s[68:71], s93 offen offset:0 sc1 lds\n"
    "v_mfma_f32_16x16x32_bf16 v[100:103], a[216:219], a[136:139], v[100:103]\n"
    "v_exp_f32_e32 v93, v93\n"
    "v_mfma_f32_16x16x32_bf16 v[100:103], a[220:223], a[140:143], v[100:103]\n"
    "ds_read_b128 a[228:231], v201 offset:0\n"
    "v_cvt_pk_bf16_f32 v90, v92, v93\n"
    "v_mfma_f32_16x16x32_bf16 v[104:107], a[192:195], a[144:147], v[48:51]\n"
    "v_exp_f32_e32 v94, v94\n"
    "v_mfma_f32_16x16x32_bf16 v[104:107], a[196:199], a[148:151], v[104:107]\n"
    "buffer_load_dwordx4 v22, s[68:71], s93 offen offset:1024 sc1 lds\n"
    "v_mfma_f32_16x16x32_bf16 v[104:107], a[200:203], a[152:155], v[104:107]\n"
    "v_exp_f32_e32 v95, v95\n"
    "v_mfma_f32_16x16x32_bf16 v[104:107], a[204:207], a[156:159], v[104:107]\n"
    "ds_read_b128 a[232:235], v202 offset:0\n"
    "v_cvt_pk_bf16_f32 v91, v94, v95\n"
    "v_mfma_f32_16x16x32_bf16 v[108:111], a[208:211], a[144:147], v[48:51]\n"
    "v_exp_f32_e32 v103, v103\n"
    "v_mfma_f32_16x16x32_bf16 v[108:111], a[212:215], a[148:151], v[108:111]\n"
    "ds_read_b128 a[236:239], v203 offset:0\n"
    "v_fma_f32 v244, v244, v138, v139\n"
    "v_mfma_f32_16x16x32_bf16 v[108:111], a[216:219], a[152:155], v[108:111]\n"
    "v_exp_f32_e32 v102, v102\n"
    "v_mfma_f32_16x16x32_bf16 v[108:111], a[220:223], a[156:159], v[108:111]\n"
    "ds_read_b128 a[240:243], v204 offset:0\n"
    "v_cvt_pk_bf16_f32 v103, v102, v103\n"
    "v_mfma_f32_16x16x32_bf16 v[112:115], a[192:195], a[160:163], v[52:55]\n"
    "buffer_load_dwordx4 v22, s[68:71], s93 offen offset:2048 sc1 lds\n"
    "v_mfma_f32_16x16x32_bf16 v[112:115], a[196:199], a[164:167], v[112:115]\n"
    "v_exp_f32_e32 v101, v101\n"
    "v_mfma_f32_16x16x32_bf16 v[112:115], a[200:203], a[168:171], v[112:115]\n"
    "buffer_load_dwordx4 v22, s[68:71], s93 offen offset:3072 sc1 lds\n"
    "v_mfma_f32_16x16x32_bf16 v[112:115], a[204:207], a[172:175], v[112:115]\n"
    "v_exp_f32_e32 v100, v100\n"
    "v_mfma_f32_16x16x32_bf16 v[116:119], a[208:211], a[160:163], v[52:55]\n"
    "ds_read_b128 a[244:247], v205 offset:0\n"
    "v_cvt_pk_bf16_f32 v102, v100, v101\n"
    "v_mfma_f32_16x16x32_bf16 v[116:119], a[212:215], a[164:167], v[116:119]\n"
    "v_exp_f32_e32 v99, v99\n"
    "v_mfma_f32_16x16x32_bf16 v[116:119], a[216:219], a[168:171], v[116:119]\n"
    "ds_read_b128 a[248:251], v206 offset:0\n"
    "v_fma_f32 v245, v245, v138, v139\n"
    "v_mfma_f32_16x16x32_bf16 v[116:119], a[220:223], a[172:175], v[116:119]\n"
    "s_add_u32 m0, s92, 0x8000\n"
    "v_exp_f32_e32 v98, v98\n"
    "v_mfma_f32_16x16x32_bf16 v[120:123], a[192:195], a[176:179], v[56:59]\n"
    "ds_read_b128 a[252:255], v207 offset:0\n"
    "v_cvt_pk_bf16_f32 v101, v98, v99\n"
    "v_mfma_f32_16x16x32_bf16 v[120:123], a[196:199], a[180:183], v[120:123]\n"
    "v_exp_f32_e32 v97, v97\n"
    "v_mfma_f32_16x16x32_bf16 v[120:123], a[200:203], a[184:187], v[120:123]\n"
    "buffer_load_dwordx4 v21, s[68:71], s93 offen offset:0 sc1 lds\n"
    "v_mfma_f32_16x16x32_bf16 v[120:123], a[204:207], a[188:191], v[120:123]\n"
    "v_exp_f32_e32 v96, v96\n"
    "v_mfma_f32_16x16x32_bf16 v[124:127], a[208:211], a[176:179], v[56:59]\n"
    "v_cvt_pk_bf16_f32 v100, v96, v97\n"
    "v_fma_f32 v246, v246, v138, v139\n"
    "v_mfma_f32_16x16x32_bf16 v[124:127], a[212:215], a[180:183], v[124:127]\n"
    "v_exp_f32_e32 v104, v104\n"
    "v_mfma_f32_16x16x32_bf16 v[124:127], a[216:219], a[184:187], v[124:127]\n"
    "buffer_load_dwordx4 v21, s[68:71], s93 offen offset:1024 sc1 lds\n"
    "v_mfma_f32_16x16x32_bf16 v[124:127], a[220:223], a[188:191], v[124:127]\n"
    "s_waitcnt lgkmcnt(0)\n"
    "v_exp_f32_e32 v105, v105\n"
    "v_mfma_f32_16x16x32_bf16 a[0:3], v[68:71], a[224:227], a[0:3]\n"
    "v_cvt_pk_bf16_f32 v104, v104, v105\n"
    "v_fma_f32 v247, v247, v138, v139\n"
    "v_mfma_f32_16x16x32_bf16 a[4:7], v[68:71], a[228:231], a[4:7]\n"
    "v_exp_f32_e32 v106, v106\n"
    "v_mfma_f32_16x16x32_bf16 a[8:11], v[68:71], a[232:235], a[8:11]\n"
    "buffer_load_dwordx4 v21, s[68:71], s93 offen offset:2048 sc1 lds\n"
    "v_mfma_f32_16x16x32_bf16 a[12:15], v[68:71], a[236:239], a[12:15]\n"
    "v_exp_f32_e32 v107, v107\n"
    "v_mfma_f32_16x16x32_bf16 a[16:19], v[68:71], a[240:243], a[16:19]\n"
    "ds_read_b128 a[192:195], v16 offset:16384\n"
    "v_cvt_pk_bf16_f32 v105, v106, v107\n"
    "v_mfma_f32_16x16x32_bf16 a[20:23], v[68:71], a[244:247], a[20:23]\n"
    "v_exp_f32_e32 v108, v108\n"
    "v_mfma_f32_16x16x32_bf16 a[24:27], v[68:71], a[248:251], a[24:27]\n"
    "buffer_load_dwordx4 v21, s[68:71], s93 offen offset:3072 sc1 lds\n"
    "v_mfma_f32_16x16x32_bf16 a[28:31], v[68:71], a[252:255], a[28:31]\n"
    "s_add_u32 s93, s93, 0x8000\n"
    "v_exp_f32_e32 v109, v109\n"
    "v_mfma_f32_16x16x32_bf16 a[32:35], v[72:75], a[224:227], a[32:35]\n"
    "ds_read_b128 a[196:199], v17 offset:16384\n"
    "v_cvt_pk_bf16_f32 v106, v108, v109\n"
    "v_mfma_f32_16x16x32_bf16 a[36:39], v[72:75], a[228:231], a[36:39]\n"
    "v_exp_f32_e32 v110, v110\n"
    "v_mfma_f32_16x16x32_bf16 a[40:43], v[72:75], a[232:235], a[40:43]\n"
    "buffer_store_dwordx4 v[188:191], v132, s[68:71], s58 offen sc1\n"
    "v_mfma_f32_16x16x32_bf16 a[44:47], v[72:75], a[236:239], a[44:47]\n"
    "s_add_u32 s58, s58, 0x10000\n"
    "v_exp_f32_e32 v111, v111\n"
    "v_mfma_f32_16x16x32_bf16 a[48:51], v[72:75], a[240:243], a[48:51]\n"
    "ds_read_b128 a[200:203], v18 offset:16384\n"
    "v_cvt_pk_bf16_f32 v107, v110, v111\n"
    "v_mfma_f32_16x16x32_bf16 a[52:55], v[72:75], a[244:247], a[52:55]\n"
    "v_exp_f32_e32 v119, v119\n"
    "v_mfma_f32_16x16x32_bf16 a[56:59], v[72:75], a[248:251], a[56:59]\n"
    "buffer_load_dword v23, v61, s[88:91], 0 offen sc1\n"
    "v_mfma_f32_16x16x32_bf16 a[60:63], v[72:75], a[252:255], a[60:63]\n"
    "v_exp_f32_e32 v118, v118\n"
    "v_smfmac_f32_16x16x64_bf16 v[232:235], v[224:227], v[68:75], v228\n"
    "ds_read_b128 a[204:207], v19 offset:16384\n"
    "v_cvt_pk_bf16_f32 v119, v118, v119\n"
    "v_mfma_f32_16x16x32_bf16 a[64:67], v[84:87], a[224:227], a[64:67]\n"
    "ds_read_b128 a[208:211], v16 offset:20480\n"
    "v_lshlrev_b32_e32 v156, 16, v248\n"
    "v_mfma_f32_16x16x32_bf16 a[68:71], v[84:87], a[228:231], a[68:71]\n"
    "v_exp_f32_e32 v117, v117\n"
    "v_mfma_f32_16x16x32_bf16 a[72:75], v[84:87], a[232:235], a[72:75]\n"
    "ds_read_b128 a[212:215], v17 offset:20480\n"
    "v_and_b32_e32 v157, s95, v248\n"
    "v_mfma_f32_16x16x32_bf16 a[76:79], v[84:87], a[236:239], a[76:79]\n"
    "v_exp_f32_e32 v116, v116\n"
    "v_mfma_f32_16x16x32_bf16 a[80:83], v[84:87], a[240:243], a[80:83]\n"
    "ds_read_b128 a[216:219], v18 offset:20480\n"
    "v_cvt_pk_bf16_f32 v118, v116, v117\n"
    "v_mfma_f32_16x16x32_bf16 a[84:87], v[84:87], a[244:247], a[84:87]\n"
    "v_exp_f32_e32 v115, v115\n"
    "v_mfma_f32_16x16x32_bf16 a[88:91], v[84:87], a[248:251], a[88:91]\n"
    "ds_read_b128 a[220:223], v19 offset:20480\n"
    "v_lshl_add_u32 v158, v252, 16, v137\n"
    "v_mfma_f32_16x16x32_bf16 a[92:95], v[84:87], a[252:255], a[92:95]\n"
    "v_exp_f32_e32 v114, v114\n"
    "v_mfma_f32_16x16x32_bf16 a[96:99], v[88:91], a[224:227], a[96:99]\n"
    "v_cvt_pk_bf16_f32 v117, v114, v115\n"
    "v_bitop3_b32 v159, v252, s95, v137 bitop3:0x6a\n"
    "v_mfma_f32_16x16x32_bf16 a[100:103], v[88:91], a[228:231], a[100:103]\n"
    "v_exp_f32_e32 v113, v113\n"
    "v_mfma_f32_16x16x32_bf16 a[104:107], v[88:91], a[232:235], a[104:107]\n"
    "v_mul_f32_e32 v156, v240, v156\n"
    "v_mul_f32_e32 v157, v241, v157\n"
    "v_mfma_f32_16x16x32_bf16 a[108:111], v[88:91], a[236:239], a[108:111]\n"
    "v_exp_f32_e32 v112, v112\n"
    "v_mfma_f32_16x16x32_bf16 a[112:115], v[88:91], a[240:243], a[112:115]\n"
    "v_cvt_pk_bf16_f32 v116, v112, v113\n"
    "v_fmac_f32_dpp v156, v240, v158 quad_perm:[1,0,3,2] row_mask:0xf bank_mask:0xf\n"
    "v_mfma_f32_16x16x32_bf16 a[116:119], v[88:91], a[244:247], a[116:119]\n"
    "v_exp_f32_e32 v120, v120\n"
    "v_mfma_f32_16x16x32_bf16 a[120:123], v[88:91], a[248:251], a[120:123]\n"
    "v_fmac_f32_dpp v157, v241, v159 quad_perm:[1,0,3,2] row_mask:0xf bank_mask:0xf\n"
    "v_cvt_pk_bf16_f32 v160, v156, v157\n"
    "v_mfma_f32_16x16x32_bf16 a[124:127], v[88:91], a[252:255], a[124:127]\n"
    "v_exp_f32_e32 v121, v121\n"
    "v_smfmac_f32_16x16x64_bf16 v[236:239], v[224:227], v[84:91], v228\n"
    "s_waitcnt lgkmcnt(0)\n"
    "v_cvt_pk_bf16_f32 v120, v120, v121\n"
    "v_lshlrev_b32_e32 v156, 16, v249\n"
    "v_mfma_f32_16x16x32_bf16 v[64:67], a[192:195], a[128:131], v[44:47]\n"
    "v_exp_f32_e32 v122, v122\n"
    "v_mfma_f32_16x16x32_bf16 v[64:67], a[196:199], a[132:135], v[64:67]\n"
    "ds_read_b128 a[224:227], v200 offset:8192\n"
    "v_mfma_f32_16x16x32_bf16 v[64:67], a[200:203], a[136:139], v[64:67]\n"
    "v_exp_f32_e32 v123, v123\n"
    "v_mfma_f32_16x16x32_bf16 v[64:67], a[204:207], a[140:143], v[64:67]\n"
    "ds_read_b128 a[228:231], v201 offset:8192\n"
    "v_cvt_pk_bf16_f32 v121, v122, v123\n"
    "v_mfma_f32_16x16x32_bf16 v[68:71], a[208:211], a[128:131], v[44:47]\n"
    "v_exp_f32_e32 v124, v124\n"
    "v_mfma_f32_16x16x32_bf16 v[68:71], a[212:215], a[132:135], v[68:71]\n"
    "ds_read_b128 a[232:235], v202 offset:8192\n"
    "v_and_b32_e32 v157, s95, v249\n"
    "v_mfma_f32_16x16x32_bf16 v[68:71], a[216:219], a[136:139], v[68:71]\n"
    "v_exp_f32_e32 v125, v125\n"
    "v_mfma_f32_16x16x32_bf16 v[68:71], a[220:223], a[140:143], v[68:71]\n"
    "ds_read_b128 a[236:239], v203 offset:8192\n"
    "v_cvt_pk_bf16_f32 v122, v124, v125\n"
    "v_mfma_f32_16x16x32_bf16 v[72:75], a[192:195], a[144:147], v[48:51]\n"
    "v_exp_f32_e32 v126, v126\n"
    "v_mfma_f32_16x16x32_bf16 v[72:75], a[196:199], a[148:151], v[72:75]\n"
    "ds_read_b128 a[240:243], v204 offset:8192\n"
    "v_lshl_add_u32 v158, v253, 16, v137\n"
    "v_mfma_f32_16x16x32_bf16 v[72:75], a[200:203], a[152:155], v[72:75]\n"
    "v_exp_f32_e32 v127, v127\n"
    "v_mfma_f32_16x16x32_bf16 v[72:75], a[204:207], a[156:159], v[72:75]\n"
    "ds_read_b128 a[244:247], v205 offset:8192\n"
    "v_cvt_pk_bf16_f32 v123, v126, v127\n"
    "v_mfma_f32_16x16x32_bf16 v[76:79], a[208:211], a[144:147], v[48:51]\n"
    "v_exp_f32_e32 v71, v71\n"
    "v_mfma_f32_16x16x32_bf16 v[76:79], a[212:215], a[148:151], v[76:79]\n"
    "ds_read_b128 a[248:251], v206 offset:8192\n"
    "v_bitop3_b32 v159, v253, s95, v137 bitop3:0x6a\n"
    "v_mfma_f32_16x16x32_bf16 v[76:79], a[216:219], a[152:155], v[76:79]\n"
    "v_exp_f32_e32 v70, v70\n"
    "v_mfma_f32_16x16x32_bf16 v[76:79], a[220:223], a[156:159], v[76:79]\n"
    "ds_read_b128 a[252:255], v207 offset:8192\n"
    "v_cvt_pk_bf16_f32 v71, v70, v71\n"
    "v_mfma_f32_16x16x32_bf16 v[80:83], a[192:195], a[160:163], v[52:55]\n"
    "v_mul_f32_e32 v156, v242, v156\n"
    "v_mul_f32_e32 v157, v243, v157\n"
    "v_mfma_f32_16x16x32_bf16 v[80:83], a[196:199], a[164:167], v[80:83]\n"
    "v_exp_f32_e32 v69, v69\n"
    "v_mfma_f32_16x16x32_bf16 v[80:83], a[200:203], a[168:171], v[80:83]\n"
    "v_fmac_f32_dpp v156, v242, v158 quad_perm:[1,0,3,2] row_mask:0xf bank_mask:0xf\n"
    "v_fmac_f32_dpp v157, v243, v159 quad_perm:[1,0,3,2] row_mask:0xf bank_mask:0xf\n"
    "v_mfma_f32_16x16x32_bf16 v[80:83], a[204:207], a[172:175], v[80:83]\n"
    "v_exp_f32_e32 v68, v68\n"
    "v_mfma_f32_16x16x32_bf16 v[84:87], a[208:211], a[160:163], v[52:55]\n"
    "v_cvt_pk_bf16_f32 v70, v68, v69\n"
    "v_cvt_pk_bf16_f32 v161, v156, v157\n"
    "v_mfma_f32_16x16x32_bf16 v[84:87], a[212:215], a[164:167], v[84:87]\n"
    "v_exp_f32_e32 v67, v67\n"
    "v_mfma_f32_16x16x32_bf16 v[84:87], a[216:219], a[168:171], v[84:87]\n"
    "v_lshlrev_b32_e32 v156, 16, v250\n"
    "v_and_b32_e32 v157, s95, v250\n"
    "v_mfma_f32_16x16x32_bf16 v[84:87], a[220:223], a[172:175], v[84:87]\n"
    "v_exp_f32_e32 v66, v66\n"
    "v_mfma_f32_16x16x32_bf16 v[88:91], a[192:195], a[176:179], v[56:59]\n"
    "v_cvt_pk_bf16_f32 v69, v66, v67\n"
    "v_lshl_add_u32 v158, v254, 16, v137\n"
    "v_mfma_f32_16x16x32_bf16 v[88:91], a[196:199], a[180:183], v[88:91]\n"
    "v_exp_f32_e32 v65, v65\n"
    "v_mfma_f32_16x16x32_bf16 v[88:91], a[200:203], a[184:187], v[88:91]\n"
    "v_bitop3_b32 v159, v254, s95, v137 bitop3:0x6a\n"
    "v_mul_f32_e32 v156, v244, v156\n"
    "v_mfma_f32_16x16x32_bf16 v[88:91], a[204:207], a[188:191], v[88:91]\n"
    "v_exp_f32_e32 v64, v64\n"
    "v_mfma_f32_16x16x32_bf16 v[92:95], a[208:211], a[176:179], v[56:59]\n"
    "v_cvt_pk_bf16_f32 v68, v64, v65\n"
    "v_mfma_f32_16x16x32_bf16 v[92:95], a[212:215], a[180:183], v[92:95]\n"
    "v_exp_f32_e32 v72, v72\n"
    "v_mfma_f32_16x16x32_bf16 v[92:95], a[216:219], a[184:187], v[92:95]\n"
    "v_mul_f32_e32 v157, v245, v157\n"
    "v_fmac_f32_dpp v156, v244, v158 quad_perm:[1,0,3,2] row_mask:0xf bank_mask:0xf\n"
    "v_mfma_f32_16x16x32_bf16 v[92:95], a[220:223], a[188:191], v[92:95]\n"
    "s_waitcnt lgkmcnt(0)\n"
    "v_exp_f32_e32 v73, v73\n"
    "v_mfma_f32_16x16x32_bf16 a[0:3], v[100:103], a[224:227], a[0:3]\n"
    "v_cvt_pk_bf16_f32 v72, v72, v73\n"
    "v_fmac_f32_dpp v157, v245, v159 quad_perm:[1,0,3,2] row_mask:0xf bank_mask:0xf\n"
    "v_mfma_f32_16x16x32_bf16 a[4:7], v[100:103], a[228:231], a[4:7]\n"
    "v_exp_f32_e32 v74, v74\n"
    "v_mfma_f32_16x16x32_bf16 a[8:11], v[100:103], a[232:235], a[8:11]\n"
    "ds_read_b128 a[192:195], v16 offset:24576\n"
    "v_cvt_pk_bf16_f32 v162, v156, v157\n"
    "v_mfma_f32_16x16x32_bf16 a[12:15], v[100:103], a[236:239], a[12:15]\n"
    "v_exp_f32_e32 v75, v75\n"
    "v_mfma_f32_16x16x32_bf16 a[16:19], v[100:103], a[240:243], a[16:19]\n"
    "ds_read_b128 a[196:199], v17 offset:24576\n"
    "v_cvt_pk_bf16_f32 v73, v74, v75\n"
    "v_mfma_f32_16x16x32_bf16 a[20:23], v[100:103], a[244:247], a[20:23]\n"
    "v_exp_f32_e32 v76, v76\n"
    "v_mfma_f32_16x16x32_bf16 a[24:27], v[100:103], a[248:251], a[24:27]\n"
    "ds_read_b128 a[200:203], v18 offset:24576\n"
    "v_lshlrev_b32_e32 v156, 16, v251\n"
    "v_mfma_f32_16x16x32_bf16 a[28:31], v[100:103], a[252:255], a[28:31]\n"
    "v_exp_f32_e32 v77, v77\n"
    "v_mfma_f32_16x16x32_bf16 a[32:35], v[104:107], a[224:227], a[32:35]\n"
    "ds_read_b128 a[204:207], v19 offset:24576\n"
    "v_cvt_pk_bf16_f32 v74, v76, v77\n"
    "v_mfma_f32_16x16x32_bf16 a[36:39], v[104:107], a[228:231], a[36:39]\n"
    "v_exp_f32_e32 v78, v78\n"
    "v_mfma_f32_16x16x32_bf16 a[40:43], v[104:107], a[232:235], a[40:43]\n"
    "ds_read_b128 a[208:211], v16 offset:28672\n"
    "v_and_b32_e32 v157, s95, v251\n"
    "v_mfma_f32_16x16x32_bf16 a[44:47], v[104:107], a[236:239], a[44:47]\n"
    "v_exp_f32_e32 v79, v79\n"
    "v_mfma_f32_16x16x32_bf16 a[48:51], v[104:107], a[240:243], a[48:51]\n"
    "ds_read_b128 a[212:215], v17 offset:28672\n"
    "v_cvt_pk_bf16_f32 v75, v78, v79\n"
    "v_mfma_f32_16x16x32_bf16 a[52:55], v[104:107], a[244:247], a[52:55]\n"
    "v_exp_f32_e32 v87, v87\n"
    "v_mfma_f32_16x16x32_bf16 a[56:59], v[104:107], a[248:251], a[56:59]\n"
    "s_waitcnt vmcnt(0)\n"
    "ds_read_b128 a[216:219], v18 offset:28672\n"
    "v_max_u32_e32 v23, s96, v23\n"
    "v_mfma_f32_16x16x32_bf16 a[60:63], v[104:107], a[252:255], a[60:63]\n"
    "v_exp_f32_e32 v86, v86\n"
    "v_smfmac_f32_16x16x64_bf16 v[232:235], v[224:227], v[100:107], v228\n"
    "ds_write_b32 v229, v23\n"
    "v_cvt_pk_bf16_f32 v87, v86, v87\n"
    "v_mfma_f32_16x16x32_bf16 a[64:67], v[116:119], a[224:227], a[64:67]\n"
    "ds_read_b128 a[220:223], v19 offset:28672\n"
    "v_lshl_add_u32 v158, v255, 16, v137\n"
    "v_mfma_f32_16x16x32_bf16 a[68:71], v[116:119], a[228:231], a[68:71]\n"
    "v_exp_f32_e32 v85, v85\n"
    "v_mfma_f32_16x16x32_bf16 a[72:75], v[116:119], a[232:235], a[72:75]\n"
    "ds_read_b128 v[180:183], v134\n"
    "v_bitop3_b32 v159, v255, s95, v137 bitop3:0x6a\n"
    "v_mfma_f32_16x16x32_bf16 a[76:79], v[116:119], a[236:239], a[76:79]\n"
    "v_exp_f32_e32 v84, v84\n"
    "v_mfma_f32_16x16x32_bf16 a[80:83], v[116:119], a[240:243], a[80:83]\n"
    "v_cvt_pk_bf16_f32 v86, v84, v85\n"
    "v_add_u32_e32 v134, 0x200, v134\n"
    "v_mfma_f32_16x16x32_bf16 a[84:87], v[116:119], a[244:247], a[84:87]\n"
    "v_exp_f32_e32 v83, v83\n"
    "v_mfma_f32_16x16x32_bf16 a[88:91], v[116:119], a[248:251], a[88:91]\n"
    "ds_read_b32 v231, v230\n"
    "v_mov_b32_e32 v62, s60\n"
    "v_mfma_f32_16x16x32_bf16 a[92:95], v[116:119], a[252:255], a[92:95]\n"
    "v_exp_f32_e32 v82, v82\n"
    "v_mfma_f32_16x16x32_bf16 a[96:99], v[120:123], a[224:227], a[96:99]\n"
    "v_cvt_pk_bf16_f32 v85, v82, v83\n"
    "v_mul_f32_e32 v156, v246, v156\n"
    "v_mfma_f32_16x16x32_bf16 a[100:103], v[120:123], a[228:231], a[100:103]\n"
    "v_exp_f32_e32 v81, v81\n"
    "v_mfma_f32_16x16x32_bf16 a[104:107], v[120:123], a[232:235], a[104:107]\n"
    "v_mul_f32_e32 v157, v247, v157\n"
    "v_fmac_f32_dpp v156, v246, v158 quad_perm:[1,0,3,2] row_mask:0xf bank_mask:0xf\n"
    "v_mfma_f32_16x16x32_bf16 a[108:111], v[120:123], a[236:239], a[108:111]\n"
    "v_exp_f32_e32 v80, v80\n"
    "v_mfma_f32_16x16x32_bf16 a[112:115], v[120:123], a[240:243], a[112:115]\n"
    "v_cvt_pk_bf16_f32 v84, v80, v81\n"
    "v_fmac_f32_dpp v157, v247, v159 quad_perm:[1,0,3,2] row_mask:0xf bank_mask:0xf\n"
    "v_mfma_f32_16x16x32_bf16 a[116:119], v[120:123], a[244:247], a[116:119]\n"
    "v_exp_f32_e32 v88, v88\n"
    "v_mfma_f32_16x16x32_bf16 a[120:123], v[120:123], a[248:251], a[120:123]\n"
    "s_waitcnt lgkmcnt(0)\n"
    "v_cmp_lt_u32_e32 vcc, s96, v231\n"
    "v_cvt_pk_bf16_f32 v163, v156, v157\n"
    "v_mfma_f32_16x16x32_bf16 a[124:127], v[120:123], a[252:255], a[124:127]\n"
    "v_exp_f32_e32 v89, v89\n"
    "v_smfmac_f32_16x16x64_bf16 v[236:239], v[224:227], v[116:123], v228\n"
    "s_cbranch_vccnz .Lspin_p24\n"
    ".Lback_p24:\n"
    "v_cvt_pk_bf16_f32 v88, v88, v89\n"
    "v_mfma_f32_16x16x32_bf16 v[96:99], a[192:195], a[128:131], v[44:47]\n"
    "v_exp_f32_e32 v90, v90\n"
    "v_mfma_f32_16x16x32_bf16 v[96:99], a[196:199], a[132:135], v[96:99]\n"
    "buffer_store_dword v62, v60, s[88:91], 0 offen sc1\n"
    "v_mfma_f32_16x16x32_bf16 v[96:99], a[200:203], a[136:139], v[96:99]\n"
    "s_add_u32 m0, s92, 0x18000\n"
    "v_exp_f32_e32 v91, v91\n"
    "v_mfma_f32_16x16x32_bf16 v[96:99], a[204:207], a[140:143], v[96:99]\n"
    "s_sub_u32 s96, s96, 1\n"
    "ds_read_b128 a[224:227], v200 offset:16384\n"
    "v_cvt_pk_bf16_f32 v89, v90, v91\n"
    "v_mfma_f32_16x16x32_bf16 v[100:103], a[208:211], a[128:131], v[44:47]\n"
    "s_sub_u32 s60, s60, 1\n"
    "v_exp_f32_e32 v92, v92\n"
    "v_mfma_f32_16x16x32_bf16 v[100:103], a[212:215], a[132:135], v[100:103]\n"
    "buffer_load_dwordx4 v22, s[68:71], s93 offen offset:0 sc1 lds\n"
    "v_mfma_f32_16x16x32_bf16 v[100:103], a[216:219], a[136:139], v[100:103]\n"
    "v_exp_f32_e32 v93, v93\n"
    "v_mfma_f32_16x16x32_bf16 v[100:103], a[220:223], a[140:143], v[100:103]\n"
    "ds_read_b128 a[228:231], v201 offset:16384\n"
    "v_cvt_pk_bf16_f32 v90, v92, v93\n"
    "v_mfma_f32_16x16x32_bf16 v[104:107], a[192:195], a[144:147], v[48:51]\n"
    "v_exp_f32_e32 v94, v94\n"
    "v_mfma_f32_16x16x32_bf16 v[104:107], a[196:199], a[148:151], v[104:107]\n"
    "buffer_load_dwordx4 v22, s[68:71], s93 offen offset:1024 sc1 lds\n"
    "v_mfma_f32_16x16x32_bf16 v[104:107], a[200:203], a[152:155], v[104:107]\n"
    "v_exp_f32_e32 v95, v95\n"
    "v_mfma_f32_16x16x32_bf16 v[104:107], a[204:207], a[156:159], v[104:107]\n"
    "ds_read_b128 a[232:235], v202 offset:16384\n"
    "v_cvt_pk_bf16_f32 v91, v94, v95\n"
    "v_mfma_f32_16x16x32_bf16 v[108:111], a[208:211], a[144:147], v[48:51]\n"
    "v_exp_f32_e32 v103, v103\n"
    "v_mfma_f32_16x16x32_bf16 v[108:111], a[212:215], a[148:151], v[108:111]\n"
    "s_waitcnt vmcnt(29)\n"
    "ds_read_b128 a[236:239], v203 offset:16384\n"
    "v_cvt_f32_i32_e32 v172, v172\n"
    "v_mfma_f32_16x16x32_bf16 v[108:111], a[216:219], a[152:155], v[108:111]\n"
    "v_exp_f32_e32 v102, v102\n"
    "v_mfma_f32_16x16x32_bf16 v[108:111], a[220:223], a[156:159], v[108:111]\n"
    "ds_read_b128 a[240:243], v204 offset:16384\n"
    "v_cvt_pk_bf16_f32 v103, v102, v103\n"
    "v_mfma_f32_16x16x32_bf16 v[112:115], a[192:195], a[160:163], v[52:55]\n"
    "buffer_load_dwordx4 v22, s[68:71], s93 offen offset:2048 sc1 lds\n"
    "v_mfma_f32_16x16x32_bf16 v[112:115], a[196:199], a[164:167], v[112:115]\n"
    "v_exp_f32_e32 v101, v101\n"
    "v_mfma_f32_16x16x32_bf16 v[112:115], a[200:203], a[168:171], v[112:115]\n"
    "buffer_load_dwordx4 v22, s[68:71], s93 offen offset:3072 sc1 lds\n"
    "v_mfma_f32_16x16x32_bf16 v[112:115], a[204:207], a[172:175], v[112:115]\n"
    "v_exp_f32_e32 v100, v100\n"
    "v_mfma_f32_16x16x32_bf16 v[116:119], a[208:211], a[160:163], v[52:55]\n"
    "ds_read_b128 a[244:247], v205 offset:16384\n"
    "v_cvt_pk_bf16_f32 v102, v100, v101\n"
    "v_mfma_f32_16x16x32_bf16 v[116:119], a[212:215], a[164:167], v[116:119]\n"
    "v_exp_f32_e32 v99, v99\n"
    "v_mfma_f32_16x16x32_bf16 v[116:119], a[216:219], a[168:171], v[116:119]\n"
    "ds_read_b128 a[248:251], v206 offset:16384\n"
    "v_cvt_f32_i32_e32 v173, v173\n"
    "v_mfma_f32_16x16x32_bf16 v[116:119], a[220:223], a[172:175], v[116:119]\n"
    "s_add_u32 m0, s92, 0xc000\n"
    "v_exp_f32_e32 v98, v98\n"
    "v_mfma_f32_16x16x32_bf16 v[120:123], a[192:195], a[176:179], v[56:59]\n"
    "ds_read_b128 a[252:255], v207 offset:16384\n"
    "v_cvt_pk_bf16_f32 v101, v98, v99\n"
    "v_mfma_f32_16x16x32_bf16 v[120:123], a[196:199], a[180:183], v[120:123]\n"
    "v_exp_f32_e32 v97, v97\n"
    "v_mfma_f32_16x16x32_bf16 v[120:123], a[200:203], a[184:187], v[120:123]\n"
    "buffer_load_dwordx4 v21, s[68:71], s93 offen offset:0 sc1 lds\n"
    "v_mfma_f32_16x16x32_bf16 v[120:123], a[204:207], a[188:191], v[120:123]\n"
    "v_exp_f32_e32 v96, v96\n"
    "v_mfma_f32_16x16x32_bf16 v[124:127], a[208:211], a[176:179], v[56:59]\n"
    "v_cvt_pk_bf16_f32 v100, v96, v97\n"
    "v_cvt_f32_i32_e32 v174, v174\n"
    "v_mfma_f32_16x16x32_bf16 v[124:127], a[212:215], a[180:183], v[124:127]\n"
    "v_exp_f32_e32 v104, v104\n"
    "v_mfma_f32_16x16x32_bf16 v[124:127], a[216:219], a[184:187], v[124:127]\n"
    "buffer_load_dwordx4 v21, s[68:71], s93 offen offset:1024 sc1 lds\n"
    "v_mfma_f32_16x16x32_bf16 v[124:127], a[220:223], a[188:191], v[124:127]\n"
    "s_waitcnt lgkmcnt(0)\n"
    "v_exp_f32_e32 v105, v105\n"
    "v_mfma_f32_16x16x32_bf16 a[0:3], v[68:71], a[224:227], a[0:3]\n"
    "v_cvt_pk_bf16_f32 v104, v104, v105\n"
    "v_mfma_f32_16x16x32_bf16 a[4:7], v[68:71], a[228:231], a[4:7]\n"
    "v_exp_f32_e32 v106, v106\n"
    "v_mfma_f32_16x16x32_bf16 a[8:11], v[68:71], a[232:235], a[8:11]\n"
    "buffer_load_dwordx4 v21, s[68:71], s93 offen offset:2048 sc1 lds\n"
    "v_mfma_f32_16x16x32_bf16 a[12:15], v[68:71], a[236:239], a[12:15]\n"
    "v_exp_f32_e32 v107, v107\n"
    "v_mfma_f32_16x16x32_bf16 a[16:19], v[68:71], a[240:243], a[16:19]\n"
    "ds_read_b128 a[192:195], v16 offset:32768\n"
    "v_cvt_pk_bf16_f32 v105, v106, v107\n"
    "v_mfma_f32_16x16x32_bf16 a[20:23], v[68:71], a[244:247], a[20:23]\n"
    "v_exp_f32_e32 v108, v108\n"
    "v_mfma_f32_16x16x32_bf16 a[24:27], v[68:71], a[248:251], a[24:27]\n"
    "buffer_load_dwordx4 v21, s[68:71], s93 offen offset:3072 sc1 lds\n"
    "v_mfma_f32_16x16x32_bf16 a[28:31], v[68:71], a[252:255], a[28:31]\n"
    "s_add_u32 s93, s93, 0x8000\n"
    "v_exp_f32_e32 v109, v109\n"
    "v_mfma_f32_16x16x32_bf16 a[32:35], v[72:75], a[224:227], a[32:35]\n"
    "ds_read_b128 a[196:199], v17 offset:32768\n"
    "v_cvt_pk_bf16_f32 v106, v108, v109\n"
    "v_mfma_f32_16x16x32_bf16 a[36:39], v[72:75], a[228:231], a[36:39]\n"
    "v_exp_f32_e32 v110, v110\n"
    "v_mfma_f32_16x16x32_bf16 a[40:43], v[72:75], a[232:235], a[40:43]\n"
    "buffer_store_dwordx4 v[160:163], v131, s[68:71], s49 offen sc1\n"
    "v_mfma_f32_16x16x32_bf16 a[44:47], v[72:75], a[236:239], a[44:47]\n"
    "s_add_u32 s49, s49, 0x10000\n"
    "v_exp_f32_e32 v111, v111\n"
    "v_mfma_f32_16x16x32_bf16 a[48:51], v[72:75], a[240:243], a[48:51]\n"
    "ds_read_b128 a[200:203], v18 offset:32768\n"
    "v_cvt_pk_bf16_f32 v107, v110, v111\n"
    "v_mfma_f32_16x16x32_bf16 a[52:55], v[72:75], a[244:247], a[52:55]\n"
    "v_exp_f32_e32 v119, v119\n"
    "v_mfma_f32_16x16x32_bf16 a[56:59], v[72:75], a[248:251], a[56:59]\n"
    "buffer_load_dword v23, v61, s[88:91], 0 offen sc1\n"
    "v_mfma_f32_16x16x32_bf16 a[60:63], v[72:75], a[252:255], a[60:63]\n"
    "v_exp_f32_e32 v118, v118\n"
    "v_smfmac_f32_16x16x64_bf16 v[232:235], v[224:227], v[68:75], v228\n"
    "ds_read_b128 a[204:207], v19 offset:32768\n"
    "v_cvt_pk_bf16_f32 v119, v118, v119\n"
    "v_mfma_f32_16x16x32_bf16 a[64:67], v[84:87], a[224:227], a[64:67]\n"
    "ds_read_b128 a[208:211], v16 offset:36864\n"
    "v_cvt_f32_i32_e32 v175, v175\n"
    "v_mfma_f32_16x16x32_bf16 a[68:71], v[84:87], a[228:231], a[68:71]\n"
    "v_exp_f32_e32 v117, v117\n"
    "v_mfma_f32_16x16x32_bf16 a[72:75], v[84:87], a[232:235], a[72:75]\n"
    "ds_read_b128 a[212:215], v17 offset:36864\n"
    "v_cvt_f32_i32_e32 v176, v176\n"
    "v_mfma_f32_16x16x32_bf16 a[76:79], v[84:87], a[236:239], a[76:79]\n"
    "v_exp_f32_e32 v116, v116\n"
    "v_mfma_f32_16x16x32_bf16 a[80:83], v[84:87], a[240:243], a[80:83]\n"
    "ds_read_b128 a[216:219], v18 offset:36864\n"
    "v_cvt_pk_bf16_f32 v118, v116, v117\n"
    "v_mfma_f32_16x16x32_bf16 a[84:87], v[84:87], a[244:247], a[84:87]\n"
    "v_exp_f32_e32 v115, v115\n"
    "v_mfma_f32_16x16x32_bf16 a[88:91], v[84:87], a[248:251], a[88:91]\n"
    "ds_read_b128 a[220:223], v19 offset:36864\n"
    "v_mfma_f32_16x16x32_bf16 a[92:95], v[84:87], a[252:255], a[92:95]\n"
    "v_exp_f32_e32 v114, v114\n"
    "v_mfma_f32_16x16x32_bf16 a[96:99], v[88:91], a[224:227], a[96:99]\n"
    "v_cvt_pk_bf16_f32 v117, v114, v115\n"
    "v_cvt_f32_i32_e32 v177, v177\n"
    "v_mfma_f32_16x16x32_bf16 a[100:103], v[88:91], a[228:231], a[100:103]\n"
    "v_exp_f32_e32 v113, v113\n"
    "v_mfma_f32_16x16x32_bf16 a[104:107], v[88:91], a[232:235], a[104:107]\n"
    "v_cvt_f32_i32_e32 v178, v178\n"
    "v_mfma_f32_16x16x32_bf16 a[108:111], v[88:91], a[236:239], a[108:111]\n"
    "v_exp_f32_e32 v112, v112\n"
    "v_mfma_f32_16x16x32_bf16 a[112:115], v[88:91], a[240:243], a[112:115]\n"
    "v_cvt_pk_bf16_f32 v116, v112, v113\n"
    "v_cvt_f32_i32_e32 v179, v179\n"
    "v_mfma_f32_16x16x32_bf16 a[116:119], v[88:91], a[244:247], a[116:119]\n"
    "v_exp_f32_e32 v120, v120\n"
    "v_mfma_f32_16x16x32_bf16 a[120:123], v[88:91], a[248:251], a[120:123]\n"
    "v_fma_f32 v172, v172, v180, v181\n"
    "v_mfma_f32_16x16x32_bf16 a[124:127], v[88:91], a[252:255], a[124:127]\n"
    "v_exp_f32_e32 v121, v121\n"
    "v_smfmac_f32_16x16x64_bf16 v[236:239], v[224:227], v[84:91], v228\n"
    "s_waitcnt lgkmcnt(0)\n"
    "v_cvt_pk_bf16_f32 v120, v120, v121\n"
    "v_fma_f32 v173, v173, v180, v181\n"
    "v_mfma_f32_16x16x32_bf16 v[64:67], a[192:195], a[128:131], v[44:47]\n"
    "v_exp_f32_e32 v122, v122\n"
    "v_mfma_f32_16x16x32_bf16 v[64:67], a[196:199], a[132:135], v[64:67]\n"
    "ds_read_b128 a[224:227], v200 offset:24576\n"
    "v_fma_f32 v174, v174, v180, v181\n"
    "v_mfma_f32_16x16x32_bf16 v[64:67], a[200:203], a[136:139], v[64:67]\n"
    "v_exp_f32_e32 v123, v123\n"
    "v_mfma_f32_16x16x32_bf16 v[64:67], a[204:207], a[140:143], v[64:67]\n"
    "ds_read_b128 a[228:231], v201 offset:24576\n"
    "v_cvt_pk_bf16_f32 v121, v122, v123\n"
    "v_mfma_f32_16x16x32_bf16 v[68:71], a[208:211], a[128:131], v[44:47]\n"
    "v_exp_f32_e32 v124, v124\n"
    "v_mfma_f32_16x16x32_bf16 v[68:71], a[212:215], a[132:135], v[68:71]\n"
    "ds_read_b128 a[232:235], v202 offset:24576\n"
    "v_mfma_f32_16x16x32_bf16 v[68:71], a[216:219], a[136:139], v[68:71]\n"
    "v_exp_f32_e32 v125, v125\n"
    "v_mfma_f32_16x16x32_bf16 v[68:71], a[220:223], a[140:143], v[68:71]\n"
    "ds_read_b128 a[236:239], v203 offset:24576\n"
    "v_cvt_pk_bf16_f32 v122, v124, v125\n"
    "v_mfma_f32_16x16x32_bf16 v[72:75], a[192:195], a[144:147], v[48:51]\n"
    "v_exp_f32_e32 v126, v126\n"
    "v_mfma_f32_16x16x32_bf16 v[72:75], a[196:199], a[148:151], v[72:75]\n"
    "ds_read_b128 a[240:243], v204 offset:24576\n"
    "v_fma_f32 v175, v175, v180, v181\n"
    "v_mfma_f32_16x16x32_bf16 v[72:75], a[200:203], a[152:155], v[72:75]\n"
    "v_exp_f32_e32 v127, v127\n"
    "v_mfma_f32_16x16x32_bf16 v[72:75], a[204:207], a[156:159], v[72:75]\n"
    "ds_read_b128 a[244:247], v205 offset:24576\n"
    "v_cvt_pk_bf16_f32 v123, v126, v127\n"
    "v_mfma_f32_16x16x32_bf16 v[76:79], a[208:211], a[144:147], v[48:51]\n"
    "v_exp_f32_e32 v71, v71\n"
    "v_mfma_f32_16x16x32_bf16 v[76:79], a[212:215], a[148:151], v[76:79]\n"
    "ds_read_b128 a[248:251], v206 offset:24576\n"
    "v_fma_f32 v176, v176, v182, v183\n"
    "v_mfma_f32_16x16x32_bf16 v[76:79], a[216:219], a[152:155], v[76:79]\n"
    "v_exp_f32_e32 v70, v70\n"
    "v_mfma_f32_16x16x32_bf16 v[76:79], a[220:223], a[156:159], v[76:79]\n"
    "ds_read_b128 a[252:255], v207 offset:24576\n"
    "v_cvt_pk_bf16_f32 v71, v70, v71\n"
    "v_mfma_f32_16x16x32_bf16 v[80:83], a[192:195], a[160:163], v[52:55]\n"
    "v_fma_f32 v177, v177, v182, v183\n"
    "v_mfma_f32_16x16x32_bf16 v[80:83], a[196:199], a[164:167], v[80:83]\n"
    "v_exp_f32_e32 v69, v69\n"
    "v_mfma_f32_16x16x32_bf16 v[80:83], a[200:203], a[168:171], v[80:83]\n"
    "v_fma_f32 v178, v178, v182, v183\n"
    "v_mfma_f32_16x16x32_bf16 v[80:83], a[204:207], a[172:175], v[80:83]\n"
    "v_exp_f32_e32 v68, v68\n"
    "v_mfma_f32_16x16x32_bf16 v[84:87], a[208:211], a[160:163], v[52:55]\n"
    "v_cvt_pk_bf16_f32 v70, v68, v69\n"
    "v_fma_f32 v179, v179, v182, v183\n"
    "v_mfma_f32_16x16x32_bf16 v[84:87], a[212:215], a[164:167], v[84:87]\n"
    "v_exp_f32_e32 v67, v67\n"
    "v_mfma_f32_16x16x32_bf16 v[84:87], a[216:219], a[168:171], v[84:87]\n"
    "v_cvt_pk_bf16_f32 v184, v172, v173\n"
    "v_mfma_f32_16x16x32_bf16 v[84:87], a[220:223], a[172:175], v[84:87]\n"
    "v_exp_f32_e32 v66, v66\n"
    "v_mfma_f32_16x16x32_bf16 v[88:91], a[192:195], a[176:179], v[56:59]\n"
    "v_cvt_pk_bf16_f32 v69, v66, v67\n"
    "v_cvt_pk_bf16_f32 v185, v174, v175\n"
    "v_mfma_f32_16x16x32_bf16 v[88:91], a[196:199], a[180:183], v[88:91]\n"
    "v_exp_f32_e32 v65, v65\n"
    "v_mfma_f32_16x16x32_bf16 v[88:91], a[200:203], a[184:187], v[88:91]\n"
    "v_cvt_pk_bf16_f32 v186, v176, v177\n"
    "v_mfma_f32_16x16x32_bf16 v[88:91], a[204:207], a[188:191], v[88:91]\n"
    "v_exp_f32_e32 v64, v64\n"
    "v_mfma_f32_16x16x32_bf16 v[92:95], a[208:211], a[176:179], v[56:59]\n"
    "v_cvt_pk_bf16_f32 v68, v64, v65\n"
    "v_cvt_pk_bf16_f32 v187, v178, v179\n"
    "v_mfma_f32_16x16x32_bf16 v[92:95], a[212:215], a[180:183], v[92:95]\n"
    "v_exp_f32_e32 v72, v72\n"
    "v_mfma_f32_16x16x32_bf16 v[92:95], a[216:219], a[184:187], v[92:95]\n"
    "ds_write_b128 v135, v[184:187]\n"
    "v_mfma_f32_16x16x32_bf16 v[92:95], a[220:223], a[188:191], v[92:95]\n"
    "s_waitcnt lgkmcnt(1)\n"
    "v_exp_f32_e32 v73, v73\n"
    "v_mfma_f32_16x16x32_bf16 a[0:3], v[100:103], a[224:227], a[0:3]\n"
    "ds_read_b64_tr_b16 v[188:189], v136\n"
    "v_cvt_pk_bf16_f32 v72, v72, v73\n"
    "v_mfma_f32_16x16x32_bf16 a[4:7], v[100:103], a[228:231], a[4:7]\n"
    "v_exp_f32_e32 v74, v74\n"
    "v_mfma_f32_16x16x32_bf16 a[8:11], v[100:103], a[232:235], a[8:11]\n"
    "ds_read_b128 a[192:195], v16 offset:40960\n"
    "v_mfma_f32_16x16x32_bf16 a[12:15], v[100:103], a[236:239], a[12:15]\n"
    "v_exp_f32_e32 v75, v75\n"
    "v_mfma_f32_16x16x32_bf16 a[16:19], v[100:103], a[240:243], a[16:19]\n"
    "ds_read_b128 a[196:199], v17 offset:40960\n"
    "v_cvt_pk_bf16_f32 v73, v74, v75\n"
    "v_mfma_f32_16x16x32_bf16 a[20:23], v[100:103], a[244:247], a[20:23]\n"
    "v_exp_f32_e32 v76, v76\n"
    "v_mfma_f32_16x16x32_bf16 a[24:27], v[100:103], a[248:251], a[24:27]\n"
    "ds_read_b128 a[200:203], v18 offset:40960\n"
    "v_mfma_f32_16x16x32_bf16 a[28:31], v[100:103], a[252:255], a[28:31]\n"
    "v_exp_f32_e32 v77, v77\n"
    "v_mfma_f32_16x16x32_bf16 a[32:35], v[104:107], a[224:227], a[32:35]\n"
    "ds_read_b128 a[204:207], v19 offset:40960\n"
    "v_cvt_pk_bf16_f32 v74, v76, v77\n"
    "v_mfma_f32_16x16x32_bf16 a[36:39], v[104:107], a[228:231], a[36:39]\n"
    "v_exp_f32_e32 v78, v78\n"
    "v_mfma_f32_16x16x32_bf16 a[40:43], v[104:107], a[232:235], a[40:43]\n"
    "ds_read_b128 a[208:211], v16 offset:45056\n"
    "v_mfma_f32_16x16x32_bf16 a[44:47], v[104:107], a[236:239], a[44:47]\n"
    "v_exp_f32_e32 v79, v79\n"
    "v_mfma_f32_16x16x32_bf16 a[48:51], v[104:107], a[240:243], a[48:51]\n"
    "ds_read_b128 a[212:215], v17 offset:45056\n"
    "v_cvt_pk_bf16_f32 v75, v78, v79\n"
    "v_mfma_f32_16x16x32_bf16 a[52:55], v[104:107], a[244:247], a[52:55]\n"
    "v_exp_f32_e32 v87, v87\n"
    "v_mfma_f32_16x16x32_bf16 a[56:59], v[104:107], a[248:251], a[56:59]\n"
    "s_waitcnt vmcnt(0)\n"
    "ds_read_b128 a[216:219], v18 offset:45056\n"
    "v_max_u32_e32 v23, s96, v23\n"
    "v_mfma_f32_16x16x32_bf16 a[60:63], v[104:107], a[252:255], a[60:63]\n"
    "v_exp_f32_e32 v86, v86\n"
    "v_smfmac_f32_16x16x64_bf16 v[232:235], v[224:227], v[100:107], v228\n"
    "ds_write_b32 v229, v23\n"
    "v_cvt_pk_bf16_f32 v87, v86, v87\n"
    "v_mfma_f32_16x16x32_bf16 a[64:67], v[116:119], a[224:227], a[64:67]\n"
    "ds_read_b128 a[220:223], v19 offset:45056\n"
    "v_mfma_f32_16x16x32_bf16 a[68:71], v[116:119], a[228:231], a[68:71]\n"
    "v_exp_f32_e32 v85, v85\n"
    "v_mfma_f32_16x16x32_bf16 a[72:75], v[116:119], a[232:235], a[72:75]\n"
    "ds_read_b64_tr_b16 v[190:191], v136 offset:512\n"
    "v_mfma_f32_16x16x32_bf16 a[76:79], v[116:119], a[236:239], a[76:79]\n"
    "v_exp_f32_e32 v84, v84\n"
    "v_mfma_f32_16x16x32_bf16 a[80:83], v[116:119], a[240:243], a[80:83]\n"
    "v_cvt_pk_bf16_f32 v86, v84, v85\n"
    "v_mfma_f32_16x16x32_bf16 a[84:87], v[116:119], a[244:247], a[84:87]\n"
    "v_exp_f32_e32 v83, v83\n"
    "v_mfma_f32_16x16x32_bf16 a[88:91], v[116:119], a[248:251], a[88:91]\n"
    "ds_read_b32 v231, v230\n"
    "v_mov_b32_e32 v62, s60\n"
    "v_mfma_f32_16x16x32_bf16 a[92:95], v[116:119], a[252:255], a[92:95]\n"
    "v_exp_f32_e32 v82, v82\n"
    "v_mfma_f32_16x16x32_bf16 a[96:99], v[120:123], a[224:227], a[96:99]\n"
    "v_cvt_pk_bf16_f32 v85, v82, v83\n"
    "v_mfma_f32_16x16x32_bf16 a[100:103], v[120:123], a[228:231], a[100:103]\n"
    "v_exp_f32_e32 v81, v81\n"
    "v_mfma_f32_16x16x32_bf16 a[104:107], v[120:123], a[232:235], a[104:107]\n"
    "v_mfma_f32_16x16x32_bf16 a[108:111], v[120:123], a[236:239], a[108:111]\n"
    "v_exp_f32_e32 v80, v80\n"
    "v_mfma_f32_16x16x32_bf16 a[112:115], v[120:123], a[240:243], a[112:115]\n"
    "v_cvt_pk_bf16_f32 v84, v80, v81\n"
    "v_mfma_f32_16x16x32_bf16 a[116:119], v[120:123], a[244:247], a[116:119]\n"
    "v_exp_f32_e32 v88, v88\n"
    "v_mfma_f32_16x16x32_bf16 a[120:123], v[120:123], a[248:251], a[120:123]\n"
    "s_waitcnt lgkmcnt(0)\n"
    "v_cmp_lt_u32_e32 vcc, s96, v231\n"
    "v_mfma_f32_16x16x32_bf16 a[124:127], v[120:123], a[252:255], a[124:127]\n"
    "v_exp_f32_e32 v89, v89\n"
    "v_smfmac_f32_16x16x64_bf16 v[236:239], v[224:227], v[116:123], v228\n"
    "s_cbranch_vccnz .Lspin_p25\n"
    ".Lback_p25:\n"
    "v_cvt_pk_bf16_f32 v88, v88, v89\n"
    "v_mfma_f32_16x16x32_bf16 v[96:99], a[192:195], a[128:131], v[44:47]\n"
    "v_exp_f32_e32 v90, v90\n"
    "v_mfma_f32_16x16x32_bf16 v[96:99], a[196:199], a[132:135], v[96:99]\n"
    "buffer_store_dword v62, v60, s[88:91], 0 offen sc1\n"
    "v_mfma_f32_16x16x32_bf16 v[96:99], a[200:203], a[136:139], v[96:99]\n"
    "s_add_u32 m0, s92, 0x1c000\n"
    "v_exp_f32_e32 v91, v91\n"
    "v_mfma_f32_16x16x32_bf16 v[96:99], a[204:207], a[140:143], v[96:99]\n"
    "s_sub_u32 s96, s96, 1\n"
    "ds_read_b128 a[224:227], v200 offset:32768\n"
    "v_cvt_pk_bf16_f32 v89, v90, v91\n"
    "v_mfma_f32_16x16x32_bf16 v[100:103], a[208:211], a[128:131], v[44:47]\n"
    "s_sub_u32 s60, s60, 1\n"
    "v_exp_f32_e32 v92, v92\n"
    "v_mfma_f32_16x16x32_bf16 v[100:103], a[212:215], a[132:135], v[100:103]\n"
    "buffer_load_dwordx4 v22, s[68:71], s93 offen offset:0 sc1 lds\n"
    "v_mfma_f32_16x16x32_bf16 v[100:103], a[216:219], a[136:139], v[100:103]\n"
    "v_exp_f32_e32 v93, v93\n"
    "v_mfma_f32_16x16x32_bf16 v[100:103], a[220:223], a[140:143], v[100:103]\n"
    "ds_read_b128 a[228:231], v201 offset:32768\n"
    "v_cvt_pk_bf16_f32 v90, v92, v93\n"
    "v_mfma_f32_16x16x32_bf16 v[104:107], a[192:195], a[144:147], v[48:51]\n"
    "v_exp_f32_e32 v94, v94\n"
    "v_mfma_f32_16x16x32_bf16 v[104:107], a[196:199], a[148:151], v[104:107]\n"
    "buffer_load_dwordx4 v22, s[68:71], s93 offen offset:1024 sc1 lds\n"
    "v_mfma_f32_16x16x32_bf16 v[104:107], a[200:203], a[152:155], v[104:107]\n"
    "v_exp_f32_e32 v95, v95\n"
    "v_mfma_f32_16x16x32_bf16 v[104:107], a[204:207], a[156:159], v[104:107]\n"
    "ds_read_b128 a[232:235], v202 offset:32768\n"
    "v_cvt_pk_bf16_f32 v91, v94, v95\n"
    "v_mfma_f32_16x16x32_bf16 v[108:111], a[208:211], a[144:147], v[48:51]\n"
    "v_exp_f32_e32 v103, v103\n"
    "v_mfma_f32_16x16x32_bf16 v[108:111], a[212:215], a[148:151], v[108:111]\n"
    "ds_read_b128 a[236:239], v203 offset:32768\n"
    "v_mfma_f32_16x16x32_bf16 v[108:111], a[216:219], a[152:155], v[108:111]\n"
    "v_exp_f32_e32 v102, v102\n"
    "v_mfma_f32_16x16x32_bf16 v[108:111], a[220:223], a[156:159], v[108:111]\n"
    "ds_read_b128 a[240:243], v204 offset:32768\n"
    "v_cvt_pk_bf16_f32 v103, v102, v103\n"
    "v_mfma_f32_16x16x32_bf16 v[112:115], a[192:195], a[160:163], v[52:55]\n"
    "buffer_load_dwordx4 v22, s[68:71], s93 offen offset:2048 sc1 lds\n"
    "v_mfma_f32_16x16x32_bf16 v[112:115], a[196:199], a[164:167], v[112:115]\n"
    "v_exp_f32_e32 v101, v101\n"
    "v_mfma_f32_16x16x32_bf16 v[112:115], a[200:203], a[168:171], v[112:115]\n"
    "buffer_load_dwordx4 v22, s[68:71], s93 offen offset:3072 sc1 lds\n"
    "v_mfma_f32_16x16x32_bf16 v[112:115], a[204:207], a[172:175], v[112:115]\n"
    "v_exp_f32_e32 v100, v100\n"
    "v_mfma_f32_16x16x32_bf16 v[116:119], a[208:211], a[160:163], v[52:55]\n"
    "ds_read_b128 a[244:247], v205 offset:32768\n"
    "v_cvt_pk_bf16_f32 v102, v100, v101\n"
    "v_mfma_f32_16x16x32_bf16 v[116:119], a[212:215], a[164:167], v[116:119]\n"
    "v_exp_f32_e32 v99, v99\n"
    "v_mfma_f32_16x16x32_bf16 v[116:119], a[216:219], a[168:171], v[116:119]\n"
    "ds_read_b128 a[248:251], v206 offset:32768\n"
    "v_mfma_f32_16x16x32_bf16 v[116:119], a[220:223], a[172:175], v[116:119]\n"
    "s_add_u32 m0, s92, 0x0\n"
    "v_exp_f32_e32 v98, v98\n"
    "v_mfma_f32_16x16x32_bf16 v[120:123], a[192:195], a[176:179], v[56:59]\n"
    "ds_read_b128 a[252:255], v207 offset:32768\n"
    "v_cvt_pk_bf16_f32 v101, v98, v99\n"
    "v_mfma_f32_16x16x32_bf16 v[120:123], a[196:199], a[180:183], v[120:123]\n"
    "v_exp_f32_e32 v97, v97\n"
    "v_mfma_f32_16x16x32_bf16 v[120:123], a[200:203], a[184:187], v[120:123]\n"
    "buffer_load_dwordx4 v21, s[68:71], s93 offen offset:0 sc1 lds\n"
    "v_mfma_f32_16x16x32_bf16 v[120:123], a[204:207], a[188:191], v[120:123]\n"
    "v_exp_f32_e32 v96, v96\n"
    "v_mfma_f32_16x16x32_bf16 v[124:127], a[208:211], a[176:179], v[56:59]\n"
    "v_cvt_pk_bf16_f32 v100, v96, v97\n"
    "v_mfma_f32_16x16x32_bf16 v[124:127], a[212:215], a[180:183], v[124:127]\n"
    "v_exp_f32_e32 v104, v104\n"
    "v_mfma_f32_16x16x32_bf16 v[124:127], a[216:219], a[184:187], v[124:127]\n"
    "buffer_load_dwordx4 v21, s[68:71], s93 offen offset:1024 sc1 lds\n"
    "v_mfma_f32_16x16x32_bf16 v[124:127], a[220:223], a[188:191], v[124:127]\n"
    "s_waitcnt lgkmcnt(0)\n"
    "v_exp_f32_e32 v105, v105\n"
    "v_mfma_f32_16x16x32_bf16 a[0:3], v[68:71], a[224:227], a[0:3]\n"
    "v_cvt_pk_bf16_f32 v104, v104, v105\n"
    "v_mfma_f32_16x16x32_bf16 a[4:7], v[68:71], a[228:231], a[4:7]\n"
    "v_exp_f32_e32 v106, v106\n"
    "v_mfma_f32_16x16x32_bf16 a[8:11], v[68:71], a[232:235], a[8:11]\n"
    "buffer_load_dwordx4 v21, s[68:71], s93 offen offset:2048 sc1 lds\n"
    "v_mfma_f32_16x16x32_bf16 a[12:15], v[68:71], a[236:239], a[12:15]\n"
    "v_exp_f32_e32 v107, v107\n"
    "v_mfma_f32_16x16x32_bf16 a[16:19], v[68:71], a[240:243], a[16:19]\n"
    "ds_read_b128 a[192:195], v16 offset:49152\n"
    "v_cvt_pk_bf16_f32 v105, v106, v107\n"
    "v_mfma_f32_16x16x32_bf16 a[20:23], v[68:71], a[244:247], a[20:23]\n"
    "v_exp_f32_e32 v108, v108\n"
    "v_mfma_f32_16x16x32_bf16 a[24:27], v[68:71], a[248:251], a[24:27]\n"
    "buffer_load_dwordx4 v21, s[68:71], s93 offen offset:3072 sc1 lds\n"
    "v_mfma_f32_16x16x32_bf16 a[28:31], v[68:71], a[252:255], a[28:31]\n"
    "s_add_u32 s93, s93, 0x8000\n"
    "v_exp_f32_e32 v109, v109\n"
    "v_mfma_f32_16x16x32_bf16 a[32:35], v[72:75], a[224:227], a[32:35]\n"
    "ds_read_b128 a[196:199], v17 offset:49152\n"
    "v_cvt_pk_bf16_f32 v106, v108, v109\n"
    "v_mfma_f32_16x16x32_bf16 a[36:39], v[72:75], a[228:231], a[36:39]\n"
    "v_exp_f32_e32 v110, v110\n"
    "v_mfma_f32_16x16x32_bf16 a[40:43], v[72:75], a[232:235], a[40:43]\n"
    "buffer_store_dwordx4 v[188:191], v132, s[68:71], s58 offen sc1\n"
    "v_mfma_f32_16x16x32_bf16 a[44:47], v[72:75], a[236:239], a[44:47]\n"
    "s_add_u32 s58, s58, 0x10000\n"
    "v_exp_f32_e32 v111, v111\n"
    "v_mfma_f32_16x16x32_bf16 a[48:51], v[72:75], a[240:243], a[48:51]\n"
    "ds_read_b128 a[200:203], v18 offset:49152\n"
    "v_cvt_pk_bf16_f32 v107, v110, v111\n"
    "v_mfma_f32_16x16x32_bf16 a[52:55], v[72:75], a[244:247], a[52:55]\n"
    "v_exp_f32_e32 v119, v119\n"
    "v_mfma_f32_16x16x32_bf16 a[56:59], v[72:75], a[248:251], a[56:59]\n"
    "buffer_load_dword v23, v61, s[88:91], 0 offen sc1\n"
    "v_mfma_f32_16x16x32_bf16 a[60:63], v[72:75], a[252:255], a[60:63]\n"
    "v_exp_f32_e32 v118, v118\n"
    "v_smfmac_f32_16x16x64_bf16 v[232:235], v[224:227], v[68:75], v228\n"
    "ds_read_b128 a[204:207], v19 offset:49152\n"
    "v_cvt_pk_bf16_f32 v119, v118, v119\n"
    "v_mfma_f32_16x16x32_bf16 a[64:67], v[84:87], a[224:227], a[64:67]\n"
    "ds_read_b128 a[208:211], v16 offset:53248\n"
    "v_mfma_f32_16x16x32_bf16 a[68:71], v[84:87], a[228:231], a[68:71]\n"
    "v_exp_f32_e32 v117, v117\n"
    "v_mfma_f32_16x16x32_bf16 a[72:75], v[84:87], a[232:235], a[72:75]\n"
    "ds_read_b128 a[212:215], v17 offset:53248\n"
    "v_mfma_f32_16x16x32_bf16 a[76:79], v[84:87], a[236:239], a[76:79]\n"
    "v_exp_f32_e32 v116, v116\n"
    "v_mfma_f32_16x16x32_bf16 a[80:83], v[84:87], a[240:243], a[80:83]\n"
    "ds_read_b128 a[216:219], v18 offset:53248\n"
    "v_cvt_pk_bf16_f32 v118, v116, v117\n"
    "v_mfma_f32_16x16x32_bf16 a[84:87], v[84:87], a[244:247], a[84:87]\n"
    "v_exp_f32_e32 v115, v115\n"
    "v_mfma_f32_16x16x32_bf16 a[88:91], v[84:87], a[248:251], a[88:91]\n"
    "ds_read_b128 a[220:223], v19 offset:53248\n"
    "v_mfma_f32_16x16x32_bf16 a[92:95], v[84:87], a[252:255], a[92:95]\n"
    "v_exp_f32_e32 v114, v114\n"
    "v_mfma_f32_16x16x32_bf16 a[96:99], v[88:91], a[224:227], a[96:99]\n"
    "v_cvt_pk_bf16_f32 v117, v114, v115\n"
    "v_mfma_f32_16x16x32_bf16 a[100:103], v[88:91], a[228:231], a[100:103]\n"
    "v_exp_f32_e32 v113, v113\n"
    "v_mfma_f32_16x16x32_bf16 a[104:107], v[88:91], a[232:235], a[104:107]\n"
    "v_mfma_f32_16x16x32_bf16 a[108:111], v[88:91], a[236:239], a[108:111]\n"
    "v_exp_f32_e32 v112, v112\n"
    "v_mfma_f32_16x16x32_bf16 a[112:115], v[88:91], a[240:243], a[112:115]\n"
    "v_cvt_pk_bf16_f32 v116, v112, v113\n"
    "v_mfma_f32_16x16x32_bf16 a[116:119], v[88:91], a[244:247], a[116:119]\n"
    "v_exp_f32_e32 v120, v120\n"
    "v_mfma_f32_16x16x32_bf16 a[120:123], v[88:91], a[248:251], a[120:123]\n"
    "v_mfma_f32_16x16x32_bf16 a[124:127], v[88:91], a[252:255], a[124:127]\n"
    "v_exp_f32_e32 v121, v121\n"
    "v_smfmac_f32_16x16x64_bf16 v[236:239], v[224:227], v[84:91], v228\n"
    "s_waitcnt lgkmcnt(0)\n"
    "v_cvt_pk_bf16_f32 v120, v120, v121\n"
    "v_mfma_f32_16x16x32_bf16 v[64:67], a[192:195], a[128:131], v[44:47]\n"
    "v_exp_f32_e32 v122, v122\n"
    "v_mfma_f32_16x16x32_bf16 v[64:67], a[196:199], a[132:135], v[64:67]\n"
    "ds_read_b128 a[224:227], v200 offset:40960\n"
    "v_mfma_f32_16x16x32_bf16 v[64:67], a[200:203], a[136:139], v[64:67]\n"
    "v_exp_f32_e32 v123, v123\n"
    "v_mfma_f32_16x16x32_bf16 v[64:67], a[204:207], a[140:143], v[64:67]\n"
    "ds_read_b128 a[228:231], v201 offset:40960\n"
    "v_cvt_pk_bf16_f32 v121, v122, v123\n"
    "v_mfma_f32_16x16x32_bf16 v[68:71], a[208:211], a[128:131], v[44:47]\n"
    "v_exp_f32_e32 v124, v124\n"
    "v_mfma_f32_16x16x32_bf16 v[68:71], a[212:215], a[132:135], v[68:71]\n"
    "ds_read_b128 a[232:235], v202 offset:40960\n"
    "v_mfma_f32_16x16x32_bf16 v[68:71], a[216:219], a[136:139], v[68:71]\n"
    "v_exp_f32_e32 v125, v125\n"
    "v_mfma_f32_16x16x32_bf16 v[68:71], a[220:223], a[140:143], v[68:71]\n"
    "ds_read_b128 a[236:239], v203 offset:40960\n"
    "v_cvt_pk_bf16_f32 v122, v124, v125\n"
    "v_mfma_f32_16x16x32_bf16 v[72:75], a[192:195], a[144:147], v[48:51]\n"
    "v_exp_f32_e32 v126, v126\n"
    "v_mfma_f32_16x16x32_bf16 v[72:75], a[196:199], a[148:151], v[72:75]\n"
    "ds_read_b128 a[240:243], v204 offset:40960\n"
    "v_mfma_f32_16x16x32_bf16 v[72:75], a[200:203], a[152:155], v[72:75]\n"
    "v_exp_f32_e32 v127, v127\n"
    "v_mfma_f32_16x16x32_bf16 v[72:75], a[204:207], a[156:159], v[72:75]\n"
    "ds_read_b128 a[244:247], v205 offset:40960\n"
    "v_cvt_pk_bf16_f32 v123, v126, v127\n"
    "v_mfma_f32_16x16x32_bf16 v[76:79], a[208:211], a[144:147], v[48:51]\n"
    "v_exp_f32_e32 v71, v71\n"
    "v_mfma_f32_16x16x32_bf16 v[76:79], a[212:215], a[148:151], v[76:79]\n"
    "ds_read_b128 a[248:251], v206 offset:40960\n"
    "v_mfma_f32_16x16x32_bf16 v[76:79], a[216:219], a[152:155], v[76:79]\n"
    "v_exp_f32_e32 v70, v70\n"
    "v_mfma_f32_16x16x32_bf16 v[76:79], a[220:223], a[156:159], v[76:79]\n"
    "ds_read_b128 a[252:255], v207 offset:40960\n"
    "v_cvt_pk_bf16_f32 v71, v70, v71\n"
    "v_mfma_f32_16x16x32_bf16 v[80:83], a[192:195], a[160:163], v[52:55]\n"
    "v_mfma_f32_16x16x32_bf16 v[80:83], a[196:199], a[164:167], v[80:83]\n"
    "v_exp_f32_e32 v69, v69\n"
    "v_mfma_f32_16x16x32_bf16 v[80:83], a[200:203], a[168:171], v[80:83]\n"
    "v_mfma_f32_16x16x32_bf16 v[80:83], a[204:207], a[172:175], v[80:83]\n"
    "v_exp_f32_e32 v68, v68\n"
    "v_mfma_f32_16x16x32_bf16 v[84:87], a[208:211], a[160:163], v[52:55]\n"
    "v_cvt_pk_bf16_f32 v70, v68, v69\n"
    "v_mfma_f32_16x16x32_bf16 v[84:87], a[212:215], a[164:167], v[84:87]\n"
    "v_exp_f32_e32 v67, v67\n"
    "v_mfma_f32_16x16x32_bf16 v[84:87], a[216:219], a[168:171], v[84:87]\n"
    "v_mfma_f32_16x16x32_bf16 v[84:87], a[220:223], a[172:175], v[84:87]\n"
    "v_exp_f32_e32 v66, v66\n"
    "v_mfma_f32_16x16x32_bf16 v[88:91], a[192:195], a[176:179], v[56:59]\n"
    "v_cvt_pk_bf16_f32 v69, v66, v67\n"
    "v_mfma_f32_16x16x32_bf16 v[88:91], a[196:199], a[180:183], v[88:91]\n"
    "v_exp_f32_e32 v65, v65\n"
    "v_mfma_f32_16x16x32_bf16 v[88:91], a[200:203], a[184:187], v[88:91]\n"
    "v_mfma_f32_16x16x32_bf16 v[88:91], a[204:207], a[188:191], v[88:91]\n"
    "v_exp_f32_e32 v64, v64\n"
    "v_mfma_f32_16x16x32_bf16 v[92:95], a[208:211], a[176:179], v[56:59]\n"
    "v_cvt_pk_bf16_f32 v68, v64, v65\n"
    "v_mfma_f32_16x16x32_bf16 v[92:95], a[212:215], a[180:183], v[92:95]\n"
    "v_exp_f32_e32 v72, v72\n"
    "v_mfma_f32_16x16x32_bf16 v[92:95], a[216:219], a[184:187], v[92:95]\n"
    "v_mfma_f32_16x16x32_bf16 v[92:95], a[220:223], a[188:191], v[92:95]\n"
    "s_waitcnt lgkmcnt(0)\n"
    "v_exp_f32_e32 v73, v73\n"
    "v_mfma_f32_16x16x32_bf16 a[0:3], v[100:103], a[224:227], a[0:3]\n"
    "v_cvt_pk_bf16_f32 v72, v72, v73\n"
    "v_mfma_f32_16x16x32_bf16 a[4:7], v[100:103], a[228:231], a[4:7]\n"
    "v_exp_f32_e32 v74, v74\n"
    "v_mfma_f32_16x16x32_bf16 a[8:11], v[100:103], a[232:235], a[8:11]\n"
    "ds_read_b128 a[192:195], v16 offset:57344\n"
    "v_mfma_f32_16x16x32_bf16 a[12:15], v[100:103], a[236:239], a[12:15]\n"
    "v_exp_f32_e32 v75, v75\n"
    "v_mfma_f32_16x16x32_bf16 a[16:19], v[100:103], a[240:243], a[16:19]\n"
    "ds_read_b128 a[196:199], v17 offset:57344\n"
    "v_cvt_pk_bf16_f32 v73, v74, v75\n"
    "v_mfma_f32_16x16x32_bf16 a[20:23], v[100:103], a[244:247], a[20:23]\n"
    "v_exp_f32_e32 v76, v76\n"
    "v_mfma_f32_16x16x32_bf16 a[24:27], v[100:103], a[248:251], a[24:27]\n"
    "ds_read_b128 a[200:203], v18 offset:57344\n"
    "v_mfma_f32_16x16x32_bf16 a[28:31], v[100:103], a[252:255], a[28:31]\n"
    "v_exp_f32_e32 v77, v77\n"
    "v_mfma_f32_16x16x32_bf16 a[32:35], v[104:107], a[224:227], a[32:35]\n"
    "ds_read_b128 a[204:207], v19 offset:57344\n"
    "v_cvt_pk_bf16_f32 v74, v76, v77\n"
    "v_mfma_f32_16x16x32_bf16 a[36:39], v[104:107], a[228:231], a[36:39]\n"
    "v_exp_f32_e32 v78, v78\n"
    "v_mfma_f32_16x16x32_bf16 a[40:43], v[104:107], a[232:235], a[40:43]\n"
    "ds_read_b128 a[208:211], v16 offset:61440\n"
    "v_mfma_f32_16x16x32_bf16 a[44:47], v[104:107], a[236:239], a[44:47]\n"
    "v_exp_f32_e32 v79, v79\n"
    "v_mfma_f32_16x16x32_bf16 a[48:51], v[104:107], a[240:243], a[48:51]\n"
    "ds_read_b128 a[212:215], v17 offset:61440\n"
    "v_cvt_pk_bf16_f32 v75, v78, v79\n"
    "v_mfma_f32_16x16x32_bf16 a[52:55], v[104:107], a[244:247], a[52:55]\n"
    "v_exp_f32_e32 v87, v87\n"
    "v_mfma_f32_16x16x32_bf16 a[56:59], v[104:107], a[248:251], a[56:59]\n"
    "s_waitcnt vmcnt(0)\n"
    "ds_read_b128 a[216:219], v18 offset:61440\n"
    "v_max_u32_e32 v23, s96, v23\n"
    "v_mfma_f32_16x16x32_bf16 a[60:63], v[104:107], a[252:255], a[60:63]\n"
    "v_exp_f32_e32 v86, v86\n"
    "v_smfmac_f32_16x16x64_bf16 v[232:235], v[224:227], v[100:107], v228\n"
    "ds_write_b32 v229, v23\n"
    "v_cvt_pk_bf16_f32 v87, v86, v87\n"
    "v_mfma_f32_16x16x32_bf16 a[64:67], v[116:119], a[224:227], a[64:67]\n"
    "ds_read_b128 a[220:223], v19 offset:61440\n"
    "v_mfma_f32_16x16x32_bf16 a[68:71], v[116:119], a[228:231], a[68:71]\n"
    "v_exp_f32_e32 v85, v85\n"
    "v_mfma_f32_16x16x32_bf16 a[72:75], v[116:119], a[232:235], a[72:75]\n"
    "v_mfma_f32_16x16x32_bf16 a[76:79], v[116:119], a[236:239], a[76:79]\n"
    "v_exp_f32_e32 v84, v84\n"
    "v_mfma_f32_16x16x32_bf16 a[80:83], v[116:119], a[240:243], a[80:83]\n"
    "v_cvt_pk_bf16_f32 v86, v84, v85\n"
    "v_mfma_f32_16x16x32_bf16 a[84:87], v[116:119], a[244:247], a[84:87]\n"
    "v_exp_f32_e32 v83, v83\n"
    "v_mfma_f32_16x16x32_bf16 a[88:91], v[116:119], a[248:251], a[88:91]\n"
    "ds_read_b32 v231, v230\n"
    "v_mov_b32_e32 v62, s60\n"
    "v_mfma_f32_16x16x32_bf16 a[92:95], v[116:119], a[252:255], a[92:95]\n"
    "v_exp_f32_e32 v82, v82\n"
    "v_mfma_f32_16x16x32_bf16 a[96:99], v[120:123], a[224:227], a[96:99]\n"
    "v_cvt_pk_bf16_f32 v85, v82, v83\n"
    "v_mfma_f32_16x16x32_bf16 a[100:103], v[120:123], a[228:231], a[100:103]\n"
    "v_exp_f32_e32 v81, v81\n"
    "v_mfma_f32_16x16x32_bf16 a[104:107], v[120:123], a[232:235], a[104:107]\n"
    "v_mfma_f32_16x16x32_bf16 a[108:111], v[120:123], a[236:239], a[108:111]\n"
    "v_exp_f32_e32 v80, v80\n"
    "v_mfma_f32_16x16x32_bf16 a[112:115], v[120:123], a[240:243], a[112:115]\n"
    "v_cvt_pk_bf16_f32 v84, v80, v81\n"
    "v_mfma_f32_16x16x32_bf16 a[116:119], v[120:123], a[244:247], a[116:119]\n"
    "v_exp_f32_e32 v88, v88\n"
    "v_mfma_f32_16x16x32_bf16 a[120:123], v[120:123], a[248:251], a[120:123]\n"
    "s_waitcnt lgkmcnt(0)\n"
    "v_cmp_lt_u32_e32 vcc, s96, v231\n"
    "v_mfma_f32_16x16x32_bf16 a[124:127], v[120:123], a[252:255], a[124:127]\n"
    "v_exp_f32_e32 v89, v89\n"
    "v_smfmac_f32_16x16x64_bf16 v[236:239], v[224:227], v[116:123], v228\n"
    "s_cbranch_vccnz .Lspin_p26\n"
    ".Lback_p26:\n"
    "v_cvt_pk_bf16_f32 v88, v88, v89\n"
    "v_mfma_f32_16x16x32_bf16 v[96:99], a[192:195], a[128:131], v[44:47]\n"
    "v_exp_f32_e32 v90, v90\n"
    "v_mfma_f32_16x16x32_bf16 v[96:99], a[196:199], a[132:135], v[96:99]\n"
    "buffer_store_dword v62, v60, s[88:91], 0 offen sc1\n"
    "v_mfma_f32_16x16x32_bf16 v[96:99], a[200:203], a[136:139], v[96:99]\n"
    "s_add_u32 m0, s92, 0x10000\n"
    "v_exp_f32_e32 v91, v91\n"
    "v_mfma_f32_16x16x32_bf16 v[96:99], a[204:207], a[140:143], v[96:99]\n"
    "s_sub_u32 s96, s96, 1\n"
    "ds_read_b128 a[224:227], v200 offset:49152\n"
    "v_cvt_pk_bf16_f32 v89, v90, v91\n"
    "v_mfma_f32_16x16x32_bf16 v[100:103], a[208:211], a[128:131], v[44:47]\n"
    "v_exp_f32_e32 v92, v92\n"
    "v_mfma_f32_16x16x32_bf16 v[100:103], a[212:215], a[132:135], v[100:103]\n"
    "buffer_load_dwordx4 v22, s[68:71], s93 offen offset:0 sc1 lds\n"
    "v_mfma_f32_16x16x32_bf16 v[100:103], a[216:219], a[136:139], v[100:103]\n"
    "v_exp_f32_e32 v93, v93\n"
    "v_mfma_f32_16x16x32_bf16 v[100:103], a[220:223], a[140:143], v[100:103]\n"
    "ds_read_b128 a[228:231], v201 offset:49152\n"
    "v_cvt_pk_bf16_f32 v90, v92, v93\n"
    "v_mfma_f32_16x16x32_bf16 v[104:107], a[192:195], a[144:147], v[48:51]\n"
    "v_exp_f32_e32 v94, v94\n"
    "v_mfma_f32_16x16x32_bf16 v[104:107], a[196:199], a[148:151], v[104:107]\n"
    "buffer_load_dwordx4 v22, s[68:71], s93 offen offset:1024 sc1 lds\n"
    "v_mfma_f32_16x16x32_bf16 v[104:107], a[200:203], a[152:155], v[104:107]\n"
    "v_exp_f32_e32 v95, v95\n"
    "v_mfma_f32_16x16x32_bf16 v[104:107], a[204:207], a[156:159], v[104:107]\n"
    "ds_read_b128 a[232:235], v202 offset:49152\n"
    "v_cvt_pk_bf16_f32 v91, v94, v95\n"
    "v_mfma_f32_16x16x32_bf16 v[108:111], a[208:211], a[144:147], v[48:51]\n"
    "v_exp_f32_e32 v103, v103\n"
    "v_mfma_f32_16x16x32_bf16 v[108:111], a[212:215], a[148:151], v[108:111]\n"
    "ds_read_b128 a[236:239], v203 offset:49152\n"
    "v_mfma_f32_16x16x32_bf16 v[108:111], a[216:219], a[152:155], v[108:111]\n"
    "v_exp_f32_e32 v102, v102\n"
    "v_mfma_f32_16x16x32_bf16 v[108:111], a[220:223], a[156:159], v[108:111]\n"
    "ds_read_b128 a[240:243], v204 offset:49152\n"
    "v_cvt_pk_bf16_f32 v103, v102, v103\n"
    "v_mfma_f32_16x16x32_bf16 v[112:115], a[192:195], a[160:163], v[52:55]\n"
    "buffer_load_dwordx4 v22, s[68:71], s93 offen offset:2048 sc1 lds\n"
    "v_mfma_f32_16x16x32_bf16 v[112:115], a[196:199], a[164:167], v[112:115]\n"
    "v_exp_f32_e32 v101, v101\n"
    "v_mfma_f32_16x16x32_bf16 v[112:115], a[200:203], a[168:171], v[112:115]\n"
    "buffer_load_dwordx4 v22, s[68:71], s93 offen offset:3072 sc1 lds\n"
    "v_mfma_f32_16x16x32_bf16 v[112:115], a[204:207], a[172:175], v[112:115]\n"
    "v_exp_f32_e32 v100, v100\n"
    "v_mfma_f32_16x16x32_bf16 v[116:119], a[208:211], a[160:163], v[52:55]\n"
    "ds_read_b128 a[244:247], v205 offset:49152\n"
    "v_cvt_pk_bf16_f32 v102, v100, v101\n"
    "v_mfma_f32_16x16x32_bf16 v[116:119], a[212:215], a[164:167], v[116:119]\n"
    "v_exp_f32_e32 v99, v99\n"
    "v_mfma_f32_16x16x32_bf16 v[116:119], a[216:219], a[168:171], v[116:119]\n"
    "ds_read_b128 a[248:251], v206 offset:49152\n"
    "v_mfma_f32_16x16x32_bf16 v[116:119], a[220:223], a[172:175], v[116:119]\n"
    "s_add_u32 m0, s92, 0x4000\n"
    "v_exp_f32_e32 v98, v98\n"
    "v_mfma_f32_16x16x32_bf16 v[120:123], a[192:195], a[176:179], v[56:59]\n"
    "ds_read_b128 a[252:255], v207 offset:49152\n"
    "v_cvt_pk_bf16_f32 v101, v98, v99\n"
    "v_mfma_f32_16x16x32_bf16 v[120:123], a[196:199], a[180:183], v[120:123]\n"
    "v_exp_f32_e32 v97, v97\n"
    "v_mfma_f32_16x16x32_bf16 v[120:123], a[200:203], a[184:187], v[120:123]\n"
    "buffer_load_dwordx4 v21, s[68:71], s93 offen offset:0 sc1 lds\n"
    "v_mfma_f32_16x16x32_bf16 v[120:123], a[204:207], a[188:191], v[120:123]\n"
    "v_exp_f32_e32 v96, v96\n"
    "v_mfma_f32_16x16x32_bf16 v[124:127], a[208:211], a[176:179], v[56:59]\n"
    "v_cvt_pk_bf16_f32 v100, v96, v97\n"
    "v_mfma_f32_16x16x32_bf16 v[124:127], a[212:215], a[180:183], v[124:127]\n"
    "v_exp_f32_e32 v104, v104\n"
    "v_mfma_f32_16x16x32_bf16 v[124:127], a[216:219], a[184:187], v[124:127]\n"
    "buffer_load_dwordx4 v21, s[68:71], s93 offen offset:1024 sc1 lds\n"
    "v_mfma_f32_16x16x32_bf16 v[124:127], a[220:223], a[188:191], v[124:127]\n"
    "s_waitcnt lgkmcnt(0)\n"
    "v_exp_f32_e32 v105, v105\n"
    "v_mfma_f32_16x16x32_bf16 a[0:3], v[68:71], a[224:227], a[0:3]\n"
    "v_cvt_pk_bf16_f32 v104, v104, v105\n"
    "v_mfma_f32_16x16x32_bf16 a[4:7], v[68:71], a[228:231], a[4:7]\n"
    "v_exp_f32_e32 v106, v106\n"
    "v_mfma_f32_16x16x32_bf16 a[8:11], v[68:71], a[232:235], a[8:11]\n"
    "buffer_load_dwordx4 v21, s[68:71], s93 offen offset:2048 sc1 lds\n"
    "v_mfma_f32_16x16x32_bf16 a[12:15], v[68:71], a[236:239], a[12:15]\n"
    "v_exp_f32_e32 v107, v107\n"
    "v_mfma_f32_16x16x32_bf16 a[16:19], v[68:71], a[240:243], a[16:19]\n"
    "ds_read_b128 a[192:195], v16 offset:0\n"
    "v_cvt_pk_bf16_f32 v105, v106, v107\n"
    "v_mfma_f32_16x16x32_bf16 a[20:23], v[68:71], a[244:247], a[20:23]\n"
    "v_exp_f32_e32 v108, v108\n"
    "v_mfma_f32_16x16x32_bf16 a[24:27], v[68:71], a[248:251], a[24:27]\n"
    "buffer_load_dwordx4 v21, s[68:71], s93 offen offset:3072 sc1 lds\n"
    "v_mfma_f32_16x16x32_bf16 a[28:31], v[68:71], a[252:255], a[28:31]\n"
    "s_add_u32 s93, s93, 0x8000\n"
    "v_exp_f32_e32 v109, v109\n"
    "v_mfma_f32_16x16x32_bf16 a[32:35], v[72:75], a[224:227], a[32:35]\n"
    "ds_read_b128 a[196:199], v17 offset:0\n"
    "v_cvt_pk_bf16_f32 v106, v108, v109\n"
    "v_mfma_f32_16x16x32_bf16 a[36:39], v[72:75], a[228:231], a[36:39]\n"
    "v_exp_f32_e32 v110, v110\n"
    "v_mfma_f32_16x16x32_bf16 a[40:43], v[72:75], a[232:235], a[40:43]\n"
    "buffer_load_dword v23, v61, s[88:91], 0 offen sc1\n"
    "v_mfma_f32_16x16x32_bf16 a[44:47], v[72:75], a[236:239], a[44:47]\n"
    "v_exp_f32_e32 v111, v111\n"
    "v_mfma_f32_16x16x32_bf16 a[48:51], v[72:75], a[240:243], a[48:51]\n"
    "ds_read_b128 a[200:203], v18 offset:0\n"
    "v_cvt_pk_bf16_f32 v107, v110, v111\n"
    "v_mfma_f32_16x16x32_bf16 a[52:55], v[72:75], a[244:247], a[52:55]\n"
    "v_exp_f32_e32 v119, v119\n"
    "v_mfma_f32_16x16x32_bf16 a[56:59], v[72:75], a[248:251], a[56:59]\n"
    "ds_read_b128 a[204:207], v19 offset:0\n"
    "v_mfma_f32_16x16x32_bf16 a[60:63], v[72:75], a[252:255], a[60:63]\n"
    "v_exp_f32_e32 v118, v118\n"
    "v_smfmac_f32_16x16x64_bf16 v[232:235], v[224:227], v[68:75], v228\n"
    "ds_read_b128 a[208:211], v16 offset:4096\n"
    "v_cvt_pk_bf16_f32 v119, v118, v119\n"
    "v_mfma_f32_16x16x32_bf16 a[64:67], v[84:87], a[224:227], a[64:67]\n"
    "ds_read_b128 a[212:215], v17 offset:4096\n"
    "v_mfma_f32_16x16x32_bf16 a[68:71], v[84:87], a[228:231], a[68:71]\n"
    "v_exp_f32_e32 v117, v117\n"
    "v_mfma_f32_16x16x32_bf16 a[72:75], v[84:87], a[232:235], a[72:75]\n"
    "ds_read_b128 a[216:219], v18 offset:4096\n"
    "v_mfma_f32_16x16x32_bf16 a[76:79], v[84:87], a[236:239], a[76:79]\n"
    "v_exp_f32_e32 v116, v116\n"
    "v_mfma_f32_16x16x32_bf16 a[80:83], v[84:87], a[240:243], a[80:83]\n"
    "ds_read_b128 a[220:223], v19 offset:4096\n"
    "v_cvt_pk_bf16_f32 v118, v116, v117\n"
    "v_mfma_f32_16x16x32_bf16 a[84:87], v[84:87], a[244:247], a[84:87]\n"
    "v_exp_f32_e32 v115, v115\n"
    "v_mfma_f32_16x16x32_bf16 a[88:91], v[84:87], a[248:251], a[88:91]\n"
    "v_mfma_f32_16x16x32_bf16 a[92:95], v[84:87], a[252:255], a[92:95]\n"
    "v_exp_f32_e32 v114, v114\n"
    "v_mfma_f32_16x16x32_bf16 a[96:99], v[88:91], a[224:227], a[96:99]\n"
    "v_cvt_pk_bf16_f32 v117, v114, v115\n"
    "v_mfma_f32_16x16x32_bf16 a[100:103], v[88:91], a[228:231], a[100:103]\n"
    "v_exp_f32_e32 v113, v113\n"
    "v_mfma_f32_16x16x32_bf16 a[104:107], v[88:91], a[232:235], a[104:107]\n"
    "v_mfma_f32_16x16x32_bf16 a[108:111], v[88:91], a[236:239], a[108:111]\n"
    "v_exp_f32_e32 v112, v112\n"
    "v_mfma_f32_16x16x32_bf16 a[112:115], v[88:91], a[240:243], a[112:115]\n"
    "v_cvt_pk_bf16_f32 v116, v112, v113\n"
    "v_mfma_f32_16x16x32_bf16 a[116:119], v[88:91], a[244:247], a[116:119]\n"
    "v_exp_f32_e32 v120, v120\n"
    "v_mfma_f32_16x16x32_bf16 a[120:123], v[88:91], a[248:251], a[120:123]\n"
    "v_mfma_f32_16x16x32_bf16 a[124:127], v[88:91], a[252:255], a[124:127]\n"
    "v_exp_f32_e32 v121, v121\n"
    "v_smfmac_f32_16x16x64_bf16 v[236:239], v[224:227], v[84:91], v228\n"
    "s_waitcnt lgkmcnt(0)\n"
    "v_cvt_pk_bf16_f32 v120, v120, v121\n"
    "v_mfma_f32_16x16x32_bf16 v[64:67], a[192:195], a[128:131], v[44:47]\n"
    "v_exp_f32_e32 v122, v122\n"
    "v_mfma_f32_16x16x32_bf16 v[64:67], a[196:199], a[132:135], v[64:67]\n"
    "ds_read_b128 a[224:227], v200 offset:57344\n"
    "v_mfma_f32_16x16x32_bf16 v[64:67], a[200:203], a[136:139], v[64:67]\n"
    "v_exp_f32_e32 v123, v123\n"
    "v_mfma_f32_16x16x32_bf16 v[64:67], a[204:207], a[140:143], v[64:67]\n"
    "ds_read_b128 a[228:231], v201 offset:57344\n"
    "v_cvt_pk_bf16_f32 v121, v122, v123\n"
    "v_mfma_f32_16x16x32_bf16 v[68:71], a[208:211], a[128:131], v[44:47]\n"
    "v_exp_f32_e32 v124, v124\n"
    "v_mfma_f32_16x16x32_bf16 v[68:71], a[212:215], a[132:135], v[68:71]\n"
    "ds_read_b128 a[232:235], v202 offset:57344\n"
    "v_mfma_f32_16x16x32_bf16 v[68:71], a[216:219], a[136:139], v[68:71]\n"
    "v_exp_f32_e32 v125, v125\n"
    "v_mfma_f32_16x16x32_bf16 v[68:71], a[220:223], a[140:143], v[68:71]\n"
    "ds_read_b128 a[236:239], v203 offset:57344\n"
    "v_cvt_pk_bf16_f32 v122, v124, v125\n"
    "v_mfma_f32_16x16x32_bf16 v[72:75], a[192:195], a[144:147], v[48:51]\n"
    "v_exp_f32_e32 v126, v126\n"
    "v_mfma_f32_16x16x32_bf16 v[72:75], a[196:199], a[148:151], v[72:75]\n"
    "ds_read_b128 a[240:243], v204 offset:57344\n"
    "v_mfma_f32_16x16x32_bf16 v[72:75], a[200:203], a[152:155], v[72:75]\n"
    "v_exp_f32_e32 v127, v127\n"
    "v_mfma_f32_16x16x32_bf16 v[72:75], a[204:207], a[156:159], v[72:75]\n"
    "ds_read_b128 a[244:247], v205 offset:57344\n"
    "v_cvt_pk_bf16_f32 v123, v126, v127\n"
    "v_mfma_f32_16x16x32_bf16 v[76:79], a[208:211], a[144:147], v[48:51]\n"
    "v_exp_f32_e32 v71, v71\n"
    "v_mfma_f32_16x16x32_bf16 v[76:79], a[212:215], a[148:151], v[76:79]\n"
    "ds_read_b128 a[248:251], v206 offset:57344\n"
    "v_mfma_f32_16x16x32_bf16 v[76:79], a[216:219], a[152:155], v[76:79]\n"
    "v_exp_f32_e32 v70, v70\n"
    "v_mfma_f32_16x16x32_bf16 v[76:79], a[220:223], a[156:159], v[76:79]\n"
    "ds_read_b128 a[252:255], v207 offset:57344\n"
    "v_cvt_pk_bf16_f32 v71, v70, v71\n"
    "v_mfma_f32_16x16x32_bf16 v[80:83], a[192:195], a[160:163], v[52:55]\n"
    "v_mfma_f32_16x16x32_bf16 v[80:83], a[196:199], a[164:167], v[80:83]\n"
    "v_exp_f32_e32 v69, v69\n"
    "v_mfma_f32_16x16x32_bf16 v[80:83], a[200:203], a[168:171], v[80:83]\n"
    "v_mfma_f32_16x16x32_bf16 v[80:83], a[204:207], a[172:175], v[80:83]\n"
    "v_exp_f32_e32 v68, v68\n"
    "v_mfma_f32_16x16x32_bf16 v[84:87], a[208:211], a[160:163], v[52:55]\n"
    "v_cvt_pk_bf16_f32 v70, v68, v69\n"
    "v_mfma_f32_16x16x32_bf16 v[84:87], a[212:215], a[164:167], v[84:87]\n"
    "v_exp_f32_e32 v67, v67\n"
    "v_mfma_f32_16x16x32_bf16 v[84:87], a[216:219], a[168:171], v[84:87]\n"
    "v_mfma_f32_16x16x32_bf16 v[84:87], a[220:223], a[172:175], v[84:87]\n"
    "v_exp_f32_e32 v66, v66\n"
    "v_mfma_f32_16x16x32_bf16 v[88:91], a[192:195], a[176:179], v[56:59]\n"
    "v_cvt_pk_bf16_f32 v69, v66, v67\n"
    "v_mfma_f32_16x16x32_bf16 v[88:91], a[196:199], a[180:183], v[88:91]\n"
    "v_exp_f32_e32 v65, v65\n"
    "v_mfma_f32_16x16x32_bf16 v[88:91], a[200:203], a[184:187], v[88:91]\n"
    "v_mfma_f32_16x16x32_bf16 v[88:91], a[204:207], a[188:191], v[88:91]\n"
    "v_exp_f32_e32 v64, v64\n"
    "v_mfma_f32_16x16x32_bf16 v[92:95], a[208:211], a[176:179], v[56:59]\n"
    "v_cvt_pk_bf16_f32 v68, v64, v65\n"
    "v_mfma_f32_16x16x32_bf16 v[92:95], a[212:215], a[180:183], v[92:95]\n"
    "v_exp_f32_e32 v72, v72\n"
    "v_mfma_f32_16x16x32_bf16 v[92:95], a[216:219], a[184:187], v[92:95]\n"
    "v_mfma_f32_16x16x32_bf16 v[92:95], a[220:223], a[188:191], v[92:95]\n"
    "s_waitcnt lgkmcnt(0)\n"
    "v_exp_f32_e32 v73, v73\n"
    "v_mfma_f32_16x16x32_bf16 a[0:3], v[100:103], a[224:227], a[0:3]\n"
    "v_cvt_pk_bf16_f32 v72, v72, v73\n"
    "v_mfma_f32_16x16x32_bf16 a[4:7], v[100:103], a[228:231], a[4:7]\n"
    "v_exp_f32_e32 v74, v74\n"
    "v_mfma_f32_16x16x32_bf16 a[8:11], v[100:103], a[232:235], a[8:11]\n"
    "ds_read_b128 a[192:195], v16 offset:8192\n"
    "v_mfma_f32_16x16x32_bf16 a[12:15], v[100:103], a[236:239], a[12:15]\n"
    "v_exp_f32_e32 v75, v75\n"
    "v_mfma_f32_16x16x32_bf16 a[16:19], v[100:103], a[240:243], a[16:19]\n"
    "ds_read_b128 a[196:199], v17 offset:8192\n"
    "v_cvt_pk_bf16_f32 v73, v74, v75\n"
    "v_mfma_f32_16x16x32_bf16 a[20:23], v[100:103], a[244:247], a[20:23]\n"
    "v_exp_f32_e32 v76, v76\n"
    "v_mfma_f32_16x16x32_bf16 a[24:27], v[100:103], a[248:251], a[24:27]\n"
    "ds_read_b128 a[200:203], v18 offset:8192\n"
    "v_mfma_f32_16x16x32_bf16 a[28:31], v[100:103], a[252:255], a[28:31]\n"
    "v_exp_f32_e32 v77, v77\n"
    "v_mfma_f32_16x16x32_bf16 a[32:35], v[104:107], a[224:227], a[32:35]\n"
    "ds_read_b128 a[204:207], v19 offset:8192\n"
    "v_cvt_pk_bf16_f32 v74, v76, v77\n"
    "v_mfma_f32_16x16x32_bf16 a[36:39], v[104:107], a[228:231], a[36:39]\n"
    "v_exp_f32_e32 v78, v78\n"
    "v_mfma_f32_16x16x32_bf16 a[40:43], v[104:107], a[232:235], a[40:43]\n"
    "ds_read_b128 a[208:211], v16 offset:12288\n"
    "v_mfma_f32_16x16x32_bf16 a[44:47], v[104:107], a[236:239], a[44:47]\n"
    "v_exp_f32_e32 v79, v79\n"
    "v_mfma_f32_16x16x32_bf16 a[48:51], v[104:107], a[240:243], a[48:51]\n"
    "ds_read_b128 a[212:215], v17 offset:12288\n"
    "v_cvt_pk_bf16_f32 v75, v78, v79\n"
    "v_mfma_f32_16x16x32_bf16 a[52:55], v[104:107], a[244:247], a[52:55]\n"
    "v_exp_f32_e32 v87, v87\n"
    "v_mfma_f32_16x16x32_bf16 a[56:59], v[104:107], a[248:251], a[56:59]\n"
    "s_waitcnt vmcnt(0)\n"
    "ds_read_b128 a[216:219], v18 offset:12288\n"
    "v_max_u32_e32 v23, s96, v23\n"
    "v_mfma_f32_16x16x32_bf16 a[60:63], v[104:107], a[252:255], a[60:63]\n"
    "v_exp_f32_e32 v86, v86\n"
    "v_smfmac_f32_16x16x64_bf16 v[232:235], v[224:227], v[100:107], v228\n"
    "ds_write_b32 v229, v23\n"
    "v_cvt_pk_bf16_f32 v87, v86, v87\n"
    "v_mfma_f32_16x16x32_bf16 a[64:67], v[116:119], a[224:227], a[64:67]\n"
    "ds_read_b128 a[220:223], v19 offset:12288\n"
    "v_mfma_f32_16x16x32_bf16 a[68:71], v[116:119], a[228:231], a[68:71]\n"
    "v_exp_f32_e32 v85, v85\n"
    "v_mfma_f32_16x16x32_bf16 a[72:75], v[116:119], a[232:235], a[72:75]\n"
    "v_mfma_f32_16x16x32_bf16 a[76:79], v[116:119], a[236:239], a[76:79]\n"
    "v_exp_f32_e32 v84, v84\n"
    "v_mfma_f32_16x16x32_bf16 a[80:83], v[116:119], a[240:243], a[80:83]\n"
    "v_cvt_pk_bf16_f32 v86, v84, v85\n"
    "v_mfma_f32_16x16x32_bf16 a[84:87], v[116:119], a[244:247], a[84:87]\n"
    "v_exp_f32_e32 v83, v83\n"
    "v_mfma_f32_16x16x32_bf16 a[88:91], v[116:119], a[248:251], a[88:91]\n"
    "ds_read_b32 v231, v230\n"
    "v_mfma_f32_16x16x32_bf16 a[92:95], v[116:119], a[252:255], a[92:95]\n"
    "v_exp_f32_e32 v82, v82\n"
    "v_mfma_f32_16x16x32_bf16 a[96:99], v[120:123], a[224:227], a[96:99]\n"
    "v_cvt_pk_bf16_f32 v85, v82, v83\n"
    "v_mfma_f32_16x16x32_bf16 a[100:103], v[120:123], a[228:231], a[100:103]\n"
    "v_exp_f32_e32 v81, v81\n"
    "v_mfma_f32_16x16x32_bf16 a[104:107], v[120:123], a[232:235], a[104:107]\n"
    "v_mfma_f32_16x16x32_bf16 a[108:111], v[120:123], a[236:239], a[108:111]\n"
    "v_exp_f32_e32 v80, v80\n"
    "v_mfma_f32_16x16x32_bf16 a[112:115], v[120:123], a[240:243], a[112:115]\n"
    "v_cvt_pk_bf16_f32 v84, v80, v81\n"
    "v_mfma_f32_16x16x32_bf16 a[116:119], v[120:123], a[244:247], a[116:119]\n"
    "v_exp_f32_e32 v88, v88\n"
    "v_mfma_f32_16x16x32_bf16 a[120:123], v[120:123], a[248:251], a[120:123]\n"
    "s_waitcnt lgkmcnt(0)\n"
    "v_cmp_lt_u32_e32 vcc, s96, v231\n"
    "v_mfma_f32_16x16x32_bf16 a[124:127], v[120:123], a[252:255], a[124:127]\n"
    "v_exp_f32_e32 v89, v89\n"
    "v_smfmac_f32_16x16x64_bf16 v[236:239], v[224:227], v[116:123], v228\n"
    "s_cbranch_vccnz .Lspin_p27\n"
    ".Lback_p27:\n"
    "v_cvt_pk_bf16_f32 v88, v88, v89\n"
    "v_mfma_f32_16x16x32_bf16 v[96:99], a[192:195], a[128:131], v[44:47]\n"
    "v_exp_f32_e32 v90, v90\n"
    "v_mfma_f32_16x16x32_bf16 v[96:99], a[196:199], a[132:135], v[96:99]\n"
    "ds_read_b128 a[224:227], v200 offset:0\n"
    "v_mfma_f32_16x16x32_bf16 v[96:99], a[200:203], a[136:139], v[96:99]\n"
    "s_add_u32 m0, s92, 0x14000\n"
    "v_exp_f32_e32 v91, v91\n"
    "v_mfma_f32_16x16x32_bf16 v[96:99], a[204:207], a[140:143], v[96:99]\n"
    "s_sub_u32 s96, s96, 1\n"
    "ds_read_b128 a[228:231], v201 offset:0\n"
    "v_cvt_pk_bf16_f32 v89, v90, v91\n"
    "v_mfma_f32_16x16x32_bf16 v[100:103], a[208:211], a[128:131], v[44:47]\n"
    "v_exp_f32_e32 v92, v92\n"
    "v_mfma_f32_16x16x32_bf16 v[100:103], a[212:215], a[132:135], v[100:103]\n"
    "buffer_load_dwordx4 v22, s[68:71], s93 offen offset:0 sc1 lds\n"
    "v_mfma_f32_16x16x32_bf16 v[100:103], a[216:219], a[136:139], v[100:103]\n"
    "v_exp_f32_e32 v93, v93\n"
    "v_mfma_f32_16x16x32_bf16 v[100:103], a[220:223], a[140:143], v[100:103]\n"
    "ds_read_b128 a[232:235], v202 offset:0\n"
    "v_cvt_pk_bf16_f32 v90, v92, v93\n"
    "v_mfma_f32_16x16x32_bf16 v[104:107], a[192:195], a[144:147], v[48:51]\n"
    "v_exp_f32_e32 v94, v94\n"
    "v_mfma_f32_16x16x32_bf16 v[104:107], a[196:199], a[148:151], v[104:107]\n"
    "buffer_load_dwordx4 v22, s[68:71], s93 offen offset:1024 sc1 lds\n"
    "v_mfma_f32_16x16x32_bf16 v[104:107], a[200:203], a[152:155], v[104:107]\n"
    "v_exp_f32_e32 v95, v95\n"
    "v_mfma_f32_16x16x32_bf16 v[104:107], a[204:207], a[156:159], v[104:107]\n"
    "ds_read_b128 a[236:239], v203 offset:0\n"
    "v_cvt_pk_bf16_f32 v91, v94, v95\n"
    "v_mfma_f32_16x16x32_bf16 v[108:111], a[208:211], a[144:147], v[48:51]\n"
    "v_exp_f32_e32 v103, v103\n"
    "v_mfma_f32_16x16x32_bf16 v[108:111], a[212:215], a[148:151], v[108:111]\n"
    "ds_read_b128 a[240:243], v204 offset:0\n"
    "v_mfma_f32_16x16x32_bf16 v[108:111], a[216:219], a[152:155], v[108:111]\n"
    "v_exp_f32_e32 v102, v102\n"
    "v_mfma_f32_16x16x32_bf16 v[108:111], a[220:223], a[156:159], v[108:111]\n"
    "ds_read_b128 a[244:247], v205 offset:0\n"
    "v_cvt_pk_bf16_f32 v103, v102, v103\n"
    "v_mfma_f32_16x16x32_bf16 v[112:115], a[192:195], a[160:163], v[52:55]\n"
    "buffer_load_dwordx4 v22, s[68:71], s93 offen offset:2048 sc1 lds\n"
    "v_mfma_f32_16x16x32_bf16 v[112:115], a[196:199], a[164:167], v[112:115]\n"
    "v_exp_f32_e32 v101, v101\n"
    "v_mfma_f32_16x16x32_bf16 v[112:115], a[200:203], a[168:171], v[112:115]\n"
    "buffer_load_dwordx4 v22, s[68:71], s93 offen offset:3072 sc1 lds\n"
    "v_mfma_f32_16x16x32_bf16 v[112:115], a[204:207], a[172:175], v[112:115]\n"
    "v_exp_f32_e32 v100, v100\n"
    "v_mfma_f32_16x16x32_bf16 v[116:119], a[208:211], a[160:163], v[52:55]\n"
    "ds_read_b128 a[248:251], v206 offset:0\n"
    "v_cvt_pk_bf16_f32 v102, v100, v101\n"
    "v_mfma_f32_16x16x32_bf16 v[116:119], a[212:215], a[164:167], v[116:119]\n"
    "v_exp_f32_e32 v99, v99\n"
    "v_mfma_f32_16x16x32_bf16 v[116:119], a[216:219], a[168:171], v[116:119]\n"
    "ds_read_b128 a[252:255], v207 offset:0\n"
    "v_mfma_f32_16x16x32_bf16 v[116:119], a[220:223], a[172:175], v[116:119]\n"
    "s_add_u32 m0, s92, 0x8000\n"
    "v_exp_f32_e32 v98, v98\n"
    "v_mfma_f32_16x16x32_bf16 v[120:123], a[192:195], a[176:179], v[56:59]\n"
    "v_cvt_pk_bf16_f32 v101, v98, v99\n"
    "v_mfma_f32_16x16x32_bf16 v[120:123], a[196:199], a[180:183], v[120:123]\n"
    "v_exp_f32_e32 v97, v97\n"
    "v_mfma_f32_16x16x32_bf16 v[120:123], a[200:203], a[184:187], v[120:123]\n"
    "buffer_load_dwordx4 v21, s[68:71], s93 offen offset:0 sc1 lds\n"
    "v_mfma_f32_16x16x32_bf16 v[120:123], a[204:207], a[188:191], v[120:123]\n"
    "v_exp_f32_e32 v96, v96\n"
    "v_mfma_f32_16x16x32_bf16 v[124:127], a[208:211], a[176:179], v[56:59]\n"
    "v_cvt_pk_bf16_f32 v100, v96, v97\n"
    "v_mfma_f32_16x16x32_bf16 v[124:127], a[212:215], a[180:183], v[124:127]\n"
    "v_exp_f32_e32 v104, v104\n"
    "v_mfma_f32_16x16x32_bf16 v[124:127], a[216:219], a[184:187], v[124:127]\n"
    "buffer_load_dwordx4 v21, s[68:71], s93 offen offset:1024 sc1 lds\n"
    "v_mfma_f32_16x16x32_bf16 v[124:127], a[220:223], a[188:191], v[124:127]\n"
    "s_waitcnt lgkmcnt(0)\n"
    "v_exp_f32_e32 v105, v105\n"
    "v_mfma_f32_16x16x32_bf16 a[0:3], v[68:71], a[224:227], a[0:3]\n"
    "v_cvt_pk_bf16_f32 v104, v104, v105\n"
    "v_mfma_f32_16x16x32_bf16 a[4:7], v[68:71], a[228:231], a[4:7]\n"
    "v_exp_f32_e32 v106, v106\n"
    "v_mfma_f32_16x16x32_bf16 a[8:11], v[68:71], a[232:235], a[8:11]\n"
    "buffer_load_dwordx4 v21, s[68:71], s93 offen offset:2048 sc1 lds\n"
    "v_mfma_f32_16x16x32_bf16 a[12:15], v[68:71], a[236:239], a[12:15]\n"
    "v_exp_f32_e32 v107, v107\n"
    "v_mfma_f32_16x16x32_bf16 a[16:19], v[68:71], a[240:243], a[16:19]\n"
    "ds_read_b128 a[192:195], v16 offset:16384\n"
    "v_cvt_pk_bf16_f32 v105, v106, v107\n"
    "v_mfma_f32_16x16x32_bf16 a[20:23], v[68:71], a[244:247], a[20:23]\n"
    "v_exp_f32_e32 v108, v108\n"
    "v_mfma_f32_16x16x32_bf16 a[24:27], v[68:71], a[248:251], a[24:27]\n"
    "buffer_load_dwordx4 v21, s[68:71], s93 offen offset:3072 sc1 lds\n"
    "v_mfma_f32_16x16x32_bf16 a[28:31], v[68:71], a[252:255], a[28:31]\n"
    "s_add_u32 s93, s93, 0x8000\n"
    "v_exp_f32_e32 v109, v109\n"
    "v_mfma_f32_16x16x32_bf16 a[32:35], v[72:75], a[224:227], a[32:35]\n"
    "ds_read_b128 a[196:199], v17 offset:16384\n"
    "v_cvt_pk_bf16_f32 v106, v108, v109\n"
    "v_mfma_f32_16x16x32_bf16 a[36:39], v[72:75], a[228:231], a[36:39]\n"
    "v_exp_f32_e32 v110, v110\n"
    "v_mfma_f32_16x16x32_bf16 a[40:43], v[72:75], a[232:235], a[40:43]\n"
    "buffer_load_dword v23, v61, s[88:91], 0 offen sc1\n"
    "v_mfma_f32_16x16x32_bf16 a[44:47], v[72:75], a[236:239], a[44:47]\n"
    "v_exp_f32_e32 v111, v111\n"
    "v_mfma_f32_16x16x32_bf16 a[48:51], v[72:75], a[240:243], a[48:51]\n"
    "ds_read_b128 a[200:203], v18 offset:16384\n"
    "v_cvt_pk_bf16_f32 v107, v110, v111\n"
    "v_mfma_f32_16x16x32_bf16 a[52:55], v[72:75], a[244:247], a[52:55]\n"
    "v_exp_f32_e32 v119, v119\n"
    "v_mfma_f32_16x16x32_bf16 a[56:59], v[72:75], a[248:251], a[56:59]\n"
    "ds_read_b128 a[204:207], v19 offset:16384\n"
    "v_mfma_f32_16x16x32_bf16 a[60:63], v[72:75], a[252:255], a[60:63]\n"
    "v_exp_f32_e32 v118, v118\n"
    "v_smfmac_f32_16x16x64_bf16 v[232:235], v[224:227], v[68:75], v228\n"
    "ds_read_b128 a[208:211], v16 offset:20480\n"
    "v_cvt_pk_bf16_f32 v119, v118, v119\n"
    "v_mfma_f32_16x16x32_bf16 a[64:67], v[84:87], a[224:227], a[64:67]\n"
    "ds_read_b128 a[212:215], v17 offset:20480\n"
    "v_mfma_f32_16x16x32_bf16 a[68:71], v[84:87], a[228:231], a[68:71]\n"
    "v_exp_f32_e32 v117, v117\n"
    "v_mfma_f32_16x16x32_bf16 a[72:75], v[84:87], a[232:235], a[72:75]\n"
    "ds_read_b128 a[216:219], v18 offset:20480\n"
    "v_mfma_f32_16x16x32_bf16 a[76:79], v[84:87], a[236:239], a[76:79]\n"
    "v_exp_f32_e32 v116, v116\n"
    "v_mfma_f32_16x16x32_bf16 a[80:83], v[84:87], a[240:243], a[80:83]\n"
    "ds_read_b128 a[220:223], v19 offset:20480\n"
    "v_cvt_pk_bf16_f32 v118, v116, v117\n"
    "v_mfma_f32_16x16x32_bf16 a[84:87], v[84:87], a[244:247], a[84:87]\n"
    "v_exp_f32_e32 v115, v115\n"
    "v_mfma_f32_16x16x32_bf16 a[88:91], v[84:87], a[248:251], a[88:91]\n"
    "v_mfma_f32_16x16x32_bf16 a[92:95], v[84:87], a[252:255], a[92:95]\n"
    "v_exp_f32_e32 v114, v114\n"
    "v_mfma_f32_16x16x32_bf16 a[96:99], v[88:91], a[224:227], a[96:99]\n"
    "v_cvt_pk_bf16_f32 v117, v114, v115\n"
    "v_mfma_f32_16x16x32_bf16 a[100:103], v[88:91], a[228:231], a[100:103]\n"
    "v_exp_f32_e32 v113, v113\n"
    "v_mfma_f32_16x16x32_bf16 a[104:107], v[88:91], a[232:235], a[104:107]\n"
    "v_mfma_f32_16x16x32_bf16 a[108:111], v[88:91], a[236:239], a[108:111]\n"
    "v_exp_f32_e32 v112, v112\n"
    "v_mfma_f32_16x16x32_bf16 a[112:115], v[88:91], a[240:243], a[112:115]\n"
    "v_cvt_pk_bf16_f32 v116, v112, v113\n"
    "v_mfma_f32_16x16x32_bf16 a[116:119], v[88:91], a[244:247], a[116:119]\n"
    "v_exp_f32_e32 v120, v120\n"
    "v_mfma_f32_16x16x32_bf16 a[120:123], v[88:91], a[248:251], a[120:123]\n"
    "v_mfma_f32_16x16x32_bf16 a[124:127], v[88:91], a[252:255], a[124:127]\n"
    "v_exp_f32_e32 v121, v121\n"
    "v_smfmac_f32_16x16x64_bf16 v[236:239], v[224:227], v[84:91], v228\n"
    "s_waitcnt lgkmcnt(0)\n"
    "v_cvt_pk_bf16_f32 v120, v120, v121\n"
    "v_mfma_f32_16x16x32_bf16 v[64:67], a[192:195], a[128:131], v[44:47]\n"
    "v_exp_f32_e32 v122, v122\n"
    "v_mfma_f32_16x16x32_bf16 v[64:67], a[196:199], a[132:135], v[64:67]\n"
    "ds_read_b128 a[224:227], v200 offset:8192\n"
    "v_mfma_f32_16x16x32_bf16 v[64:67], a[200:203], a[136:139], v[64:67]\n"
    "v_exp_f32_e32 v123, v123\n"
    "v_mfma_f32_16x16x32_bf16 v[64:67], a[204:207], a[140:143], v[64:67]\n"
    "ds_read_b128 a[228:231], v201 offset:8192\n"
    "v_cvt_pk_bf16_f32 v121, v122, v123\n"
    "v_mfma_f32_16x16x32_bf16 v[68:71], a[208:211], a[128:131], v[44:47]\n"
    "v_exp_f32_e32 v124, v124\n"
    "v_mfma_f32_16x16x32_bf16 v[68:71], a[212:215], a[132:135], v[68:71]\n"
    "ds_read_b128 a[232:235], v202 offset:8192\n"
    "v_mfma_f32_16x16x32_bf16 v[68:71], a[216:219], a[136:139], v[68:71]\n"
    "v_exp_f32_e32 v125, v125\n"
    "v_mfma_f32_16x16x32_bf16 v[68:71], a[220:223], a[140:143], v[68:71]\n"
    "ds_read_b128 a[236:239], v203 offset:8192\n"
    "v_cvt_pk_bf16_f32 v122, v124, v125\n"
    "v_mfma_f32_16x16x32_bf16 v[72:75], a[192:195], a[144:147], v[48:51]\n"
    "v_exp_f32_e32 v126, v126\n"
    "v_mfma_f32_16x16x32_bf16 v[72:75], a[196:199], a[148:151], v[72:75]\n"
    "ds_read_b128 a[240:243], v204 offset:8192\n"
    "v_mfma_f32_16x16x32_bf16 v[72:75], a[200:203], a[152:155], v[72:75]\n"
    "v_exp_f32_e32 v127, v127\n"
    "v_mfma_f32_16x16x32_bf16 v[72:75], a[204:207], a[156:159], v[72:75]\n"
    "ds_read_b128 a[244:247], v205 offset:8192\n"
    "v_cvt_pk_bf16_f32 v123, v126, v127\n"
    "v_mfma_f32_16x16x32_bf16 v[76:79], a[208:211], a[144:147], v[48:51]\n"
    "v_exp_f32_e32 v71, v71\n"
    "v_mfma_f32_16x16x32_bf16 v[76:79], a[212:215], a[148:151], v[76:79]\n"
    "ds_read_b128 a[248:251], v206 offset:8192\n"
    "v_mfma_f32_16x16x32_bf16 v[76:79], a[216:219], a[152:155], v[76:79]\n"
    "v_exp_f32_e32 v70, v70\n"
    "v_mfma_f32_16x16x32_bf16 v[76:79], a[220:223], a[156:159], v[76:79]\n"
    "ds_read_b128 a[252:255], v207 offset:8192\n"
    "v_cvt_pk_bf16_f32 v71, v70, v71\n"
    "v_mfma_f32_16x16x32_bf16 v[80:83], a[192:195], a[160:163], v[52:55]\n"
    "v_mfma_f32_16x16x32_bf16 v[80:83], a[196:199], a[164:167], v[80:83]\n"
    "v_exp_f32_e32 v69, v69\n"
    "v_mfma_f32_16x16x32_bf16 v[80:83], a[200:203], a[168:171], v[80:83]\n"
    "v_mfma_f32_16x16x32_bf16 v[80:83], a[204:207], a[172:175], v[80:83]\n"
    "v_exp_f32_e32 v68, v68\n"
    "v_mfma_f32_16x16x32_bf16 v[84:87], a[208:211], a[160:163], v[52:55]\n"
    "v_cvt_pk_bf16_f32 v70, v68, v69\n"
    "v_mfma_f32_16x16x32_bf16 v[84:87], a[212:215], a[164:167], v[84:87]\n"
    "v_exp_f32_e32 v67, v67\n"
    "v_mfma_f32_16x16x32_bf16 v[84:87], a[216:219], a[168:171], v[84:87]\n"
    "v_mfma_f32_16x16x32_bf16 v[84:87], a[220:223], a[172:175], v[84:87]\n"
    "v_exp_f32_e32 v66, v66\n"
    "v_mfma_f32_16x16x32_bf16 v[88:91], a[192:195], a[176:179], v[56:59]\n"
    "v_cvt_pk_bf16_f32 v69, v66, v67\n"
    "v_mfma_f32_16x16x32_bf16 v[88:91], a[196:199], a[180:183], v[88:91]\n"
    "v_exp_f32_e32 v65, v65\n"
    "v_mfma_f32_16x16x32_bf16 v[88:91], a[200:203], a[184:187], v[88:91]\n"
    "v_mfma_f32_16x16x32_bf16 v[88:91], a[204:207], a[188:191], v[88:91]\n"
    "v_exp_f32_e32 v64, v64\n"
    "v_mfma_f32_16x16x32_bf16 v[92:95], a[208:211], a[176:179], v[56:59]\n"
    "v_cvt_pk_bf16_f32 v68, v64, v65\n"
    "v_mfma_f32_16x16x32_bf16 v[92:95], a[212:215], a[180:183], v[92:95]\n"
    "v_exp_f32_e32 v72, v72\n"
    "v_mfma_f32_16x16x32_bf16 v[92:95], a[216:219], a[184:187], v[92:95]\n"
    "v_mfma_f32_16x16x32_bf16 v[92:95], a[220:223], a[188:191], v[92:95]\n"
    "s_waitcnt lgkmcnt(0)\n"
    "v_exp_f32_e32 v73, v73\n"
    "v_mfma_f32_16x16x32_bf16 a[0:3], v[100:103], a[224:227], a[0:3]\n"
    "v_cvt_pk_bf16_f32 v72, v72, v73\n"
    "v_mfma_f32_16x16x32_bf16 a[4:7], v[100:103], a[228:231], a[4:7]\n"
    "v_exp_f32_e32 v74, v74\n"
    "v_mfma_f32_16x16x32_bf16 a[8:11], v[100:103], a[232:235], a[8:11]\n"
    "ds_read_b128 a[192:195], v16 offset:24576\n"
    "v_mfma_f32_16x16x32_bf16 a[12:15], v[100:103], a[236:239], a[12:15]\n"
    "v_exp_f32_e32 v75, v75\n"
    "v_mfma_f32_16x16x32_bf16 a[16:19], v[100:103], a[240:243], a[16:19]\n"
    "ds_read_b128 a[196:199], v17 offset:24576\n"
    "v_cvt_pk_bf16_f32 v73, v74, v75\n"
    "v_mfma_f32_16x16x32_bf16 a[20:23], v[100:103], a[244:247], a[20:23]\n"
    "v_exp_f32_e32 v76, v76\n"
    "v_mfma_f32_16x16x32_bf16 a[24:27], v[100:103], a[248:251], a[24:27]\n"
    "ds_read_b128 a[200:203], v18 offset:24576\n"
    "v_mfma_f32_16x16x32_bf16 a[28:31], v[100:103], a[252:255], a[28:31]\n"
    "v_exp_f32_e32 v77, v77\n"
    "v_mfma_f32_16x16x32_bf16 a[32:35], v[104:107], a[224:227], a[32:35]\n"
    "ds_read_b128 a[204:207], v19 offset:24576\n"
    "v_cvt_pk_bf16_f32 v74, v76, v77\n"
    "v_mfma_f32_16x16x32_bf16 a[36:39], v[104:107], a[228:231], a[36:39]\n"
    "v_exp_f32_e32 v78, v78\n"
    "v_mfma_f32_16x16x32_bf16 a[40:43], v[104:107], a[232:235], a[40:43]\n"
    "ds_read_b128 a[208:211], v16 offset:28672\n"
    "v_mfma_f32_16x16x32_bf16 a[44:47], v[104:107], a[236:239], a[44:47]\n"
    "v_exp_f32_e32 v79, v79\n"
    "v_mfma_f32_16x16x32_bf16 a[48:51], v[104:107], a[240:243], a[48:51]\n"
    "ds_read_b128 a[212:215], v17 offset:28672\n"
    "v_cvt_pk_bf16_f32 v75, v78, v79\n"
    "v_mfma_f32_16x16x32_bf16 a[52:55], v[104:107], a[244:247], a[52:55]\n"
    "v_exp_f32_e32 v87, v87\n"
    "v_mfma_f32_16x16x32_bf16 a[56:59], v[104:107], a[248:251], a[56:59]\n"
    "s_waitcnt vmcnt(0)\n"
    "ds_read_b128 a[216:219], v18 offset:28672\n"
    "v_max_u32_e32 v23, s96, v23\n"
    "v_mfma_f32_16x16x32_bf16 a[60:63], v[104:107], a[252:255], a[60:63]\n"
    "v_exp_f32_e32 v86, v86\n"
    "v_smfmac_f32_16x16x64_bf16 v[232:235], v[224:227], v[100:107], v228\n"
    "ds_write_b32 v229, v23\n"
    "v_cvt_pk_bf16_f32 v87, v86, v87\n"
    "v_mfma_f32_16x16x32_bf16 a[64:67], v[116:119], a[224:227], a[64:67]\n"
    "ds_read_b128 a[220:223], v19 offset:28672\n"
    "v_mfma_f32_16x16x32_bf16 a[68:71], v[116:119], a[228:231], a[68:71]\n"
    "v_exp_f32_e32 v85, v85\n"
    "v_mfma_f32_16x16x32_bf16 a[72:75], v[116:119], a[232:235], a[72:75]\n"
    "v_mfma_f32_16x16x32_bf16 a[76:79], v[116:119], a[236:239], a[76:79]\n"
    "v_exp_f32_e32 v84, v84\n"
    "v_mfma_f32_16x16x32_bf16 a[80:83], v[116:119], a[240:243], a[80:83]\n"
    "v_cvt_pk_bf16_f32 v86, v84, v85\n"
    "v_mfma_f32_16x16x32_bf16 a[84:87], v[116:119], a[244:247], a[84:87]\n"
    "v_exp_f32_e32 v83, v83\n"
    "v_mfma_f32_16x16x32_bf16 a[88:91], v[116:119], a[248:251], a[88:91]\n"
    "ds_read_b32 v231, v230\n"
    "v_mfma_f32_16x16x32_bf16 a[92:95], v[116:119], a[252:255], a[92:95]\n"
    "v_exp_f32_e32 v82, v82\n"
    "v_mfma_f32_16x16x32_bf16 a[96:99], v[120:123], a[224:227], a[96:99]\n"
    "v_cvt_pk_bf16_f32 v85, v82, v83\n"
    "v_mfma_f32_16x16x32_bf16 a[100:103], v[120:123], a[228:231], a[100:103]\n"
    "v_exp_f32_e32 v81, v81\n"
    "v_mfma_f32_16x16x32_bf16 a[104:107], v[120:123], a[232:235], a[104:107]\n"
    "v_mfma_f32_16x16x32_bf16 a[108:111], v[120:123], a[236:239], a[108:111]\n"
    "v_exp_f32_e32 v80, v80\n"
    "v_mfma_f32_16x16x32_bf16 a[112:115], v[120:123], a[240:243], a[112:115]\n"
    "v_cvt_pk_bf16_f32 v84, v80, v81\n"
    "v_mfma_f32_16x16x32_bf16 a[116:119], v[120:123], a[244:247], a[116:119]\n"
    "v_exp_f32_e32 v88, v88\n"
    "v_mfma_f32_16x16x32_bf16 a[120:123], v[120:123], a[248:251], a[120:123]\n"
    "s_waitcnt lgkmcnt(0)\n"
    "v_cmp_lt_u32_e32 vcc, s96, v231\n"
    "v_mfma_f32_16x16x32_bf16 a[124:127], v[120:123], a[252:255], a[124:127]\n"
    "v_exp_f32_e32 v89, v89\n"
    "v_smfmac_f32_16x16x64_bf16 v[236:239], v[224:227], v[116:123], v228\n"
    "s_cbranch_vccnz .Lspin_p28\n"
    ".Lback_p28:\n"
    "v_cvt_pk_bf16_f32 v88, v88, v89\n"
    "v_mfma_f32_16x16x32_bf16 v[96:99], a[192:195], a[128:131], v[44:47]\n"
    "v_exp_f32_e32 v90, v90\n"
    "v_mfma_f32_16x16x32_bf16 v[96:99], a[196:199], a[132:135], v[96:99]\n"
    "ds_read_b128 a[224:227], v200 offset:16384\n"
    "v_mfma_f32_16x16x32_bf16 v[96:99], a[200:203], a[136:139], v[96:99]\n"
    "s_add_u32 m0, s92, 0x18000\n"
    "v_exp_f32_e32 v91, v91\n"
    "v_mfma_f32_16x16x32_bf16 v[96:99], a[204:207], a[140:143], v[96:99]\n"
    "ds_read_b128 a[228:231], v201 offset:16384\n"
    "v_cvt_pk_bf16_f32 v89, v90, v91\n"
    "v_mfma_f32_16x16x32_bf16 v[100:103], a[208:211], a[128:131], v[44:47]\n"
    "v_exp_f32_e32 v92, v92\n"
    "v_mfma_f32_16x16x32_bf16 v[100:103], a[212:215], a[132:135], v[100:103]\n"
    "buffer_load_dwordx4 v22, s[68:71], s93 offen offset:0 sc1 lds\n"
    "v_mfma_f32_16x16x32_bf16 v[100:103], a[216:219], a[136:139], v[100:103]\n"
    "v_exp_f32_e32 v93, v93\n"
    "v_mfma_f32_16x16x32_bf16 v[100:103], a[220:223], a[140:143], v[100:103]\n"
    "ds_read_b128 a[232:235], v202 offset:16384\n"
    "v_cvt_pk_bf16_f32 v90, v92, v93\n"
    "v_mfma_f32_16x16x32_bf16 v[104:107], a[192:195], a[144:147], v[48:51]\n"
    "v_exp_f32_e32 v94, v94\n"
    "v_mfma_f32_16x16x32_bf16 v[104:107], a[196:199], a[148:151], v[104:107]\n"
    "buffer_load_dwordx4 v22, s[68:71], s93 offen offset:1024 sc1 lds\n"
    "v_mfma_f32_16x16x32_bf16 v[104:107], a[200:203], a[152:155], v[104:107]\n"
    "v_exp_f32_e32 v95, v95\n"
    "v_mfma_f32_16x16x32_bf16 v[104:107], a[204:207], a[156:159], v[104:107]\n"
    "ds_read_b128 a[236:239], v203 offset:16384\n"
    "v_cvt_pk_bf16_f32 v91, v94, v95\n"
    "v_mfma_f32_16x16x32_bf16 v[108:111], a[208:211], a[144:147], v[48:51]\n"
    "v_exp_f32_e32 v103, v103\n"
    "v_mfma_f32_16x16x32_bf16 v[108:111], a[212:215], a[148:151], v[108:111]\n"
    "ds_read_b128 a[240:243], v204 offset:16384\n"
    "v_mfma_f32_16x16x32_bf16 v[108:111], a[216:219], a[152:155], v[108:111]\n"
    "v_exp_f32_e32 v102, v102\n"
    "v_mfma_f32_16x16x32_bf16 v[108:111], a[220:223], a[156:159], v[108:111]\n"
    "ds_read_b128 a[244:247], v205 offset:16384\n"
    "v_cvt_pk_bf16_f32 v103, v102, v103\n"
    "v_mfma_f32_16x16x32_bf16 v[112:115], a[192:195], a[160:163], v[52:55]\n"
    "buffer_load_dwordx4 v22, s[68:71], s93 offen offset:2048 sc1 lds\n"
    "v_mfma_f32_16x16x32_bf16 v[112:115], a[196:199], a[164:167], v[112:115]\n"
    "v_exp_f32_e32 v101, v101\n"
    "v_mfma_f32_16x16x32_bf16 v[112:115], a[200:203], a[168:171], v[112:115]\n"
    "buffer_load_dwordx4 v22, s[68:71], s93 offen offset:3072 sc1 lds\n"
    "v_mfma_f32_16x16x32_bf16 v[112:115], a[204:207], a[172:175], v[112:115]\n"
    "v_exp_f32_e32 v100, v100\n"
    "v_mfma_f32_16x16x32_bf16 v[116:119], a[208:211], a[160:163], v[52:55]\n"
    "ds_read_b128 a[248:251], v206 offset:16384\n"
    "v_cvt_pk_bf16_f32 v102, v100, v101\n"
    "v_mfma_f32_16x16x32_bf16 v[116:119], a[212:215], a[164:167], v[116:119]\n"
    "v_exp_f32_e32 v99, v99\n"
    "v_mfma_f32_16x16x32_bf16 v[116:119], a[216:219], a[168:171], v[116:119]\n"
    "ds_read_b128 a[252:255], v207 offset:16384\n"
    "v_mfma_f32_16x16x32_bf16 v[116:119], a[220:223], a[172:175], v[116:119]\n"
    "s_add_u32 m0, s92, 0xc000\n"
    "v_exp_f32_e32 v98, v98\n"
    "v_mfma_f32_16x16x32_bf16 v[120:123], a[192:195], a[176:179], v[56:59]\n"
    "v_cvt_pk_bf16_f32 v101, v98, v99\n"
    "v_mfma_f32_16x16x32_bf16 v[120:123], a[196:199], a[180:183], v[120:123]\n"
    "v_exp_f32_e32 v97, v97\n"
    "v_mfma_f32_16x16x32_bf16 v[120:123], a[200:203], a[184:187], v[120:123]\n"
    "buffer_load_dwordx4 v21, s[68:71], s93 offen offset:0 sc1 lds\n"
    "v_mfma_f32_16x16x32_bf16 v[120:123], a[204:207], a[188:191], v[120:123]\n"
    "v_exp_f32_e32 v96, v96\n"
    "v_mfma_f32_16x16x32_bf16 v[124:127], a[208:211], a[176:179], v[56:59]\n"
    "v_cvt_pk_bf16_f32 v100, v96, v97\n"
    "v_mfma_f32_16x16x32_bf16 v[124:127], a[212:215], a[180:183], v[124:127]\n"
    "v_exp_f32_e32 v104, v104\n"
    "v_mfma_f32_16x16x32_bf16 v[124:127], a[216:219], a[184:187], v[124:127]\n"
    "buffer_load_dwordx4 v21, s[68:71], s93 offen offset:1024 sc1 lds\n"
    "v_mfma_f32_16x16x32_bf16 v[124:127], a[220:223], a[188:191], v[124:127]\n"
    "s_waitcnt lgkmcnt(0)\n"
    "v_exp_f32_e32 v105, v105\n"
    "v_mfma_f32_16x16x32_bf16 a[0:3], v[68:71], a[224:227], a[0:3]\n"
    "v_cvt_pk_bf16_f32 v104, v104, v105\n"
    "v_mfma_f32_16x16x32_bf16 a[4:7], v[68:71], a[228:231], a[4:7]\n"
    "v_exp_f32_e32 v106, v106\n"
    "v_mfma_f32_16x16x32_bf16 a[8:11], v[68:71], a[232:235], a[8:11]\n"
    "buffer_load_dwordx4 v21, s[68:71], s93 offen offset:2048 sc1 lds\n"
    "v_mfma_f32_16x16x32_bf16 a[12:15], v[68:71], a[236:239], a[12:15]\n"
    "v_exp_f32_e32 v107, v107\n"
    "v_mfma_f32_16x16x32_bf16 a[16:19], v[68:71], a[240:243], a[16:19]\n"
    "ds_read_b128 a[192:195], v16 offset:32768\n"
    "v_cvt_pk_bf16_f32 v105, v106, v107\n"
    "v_mfma_f32_16x16x32_bf16 a[20:23], v[68:71], a[244:247], a[20:23]\n"
    "v_exp_f32_e32 v108, v108\n"
    "v_mfma_f32_16x16x32_bf16 a[24:27], v[68:71], a[248:251], a[24:27]\n"
    "buffer_load_dwordx4 v21, s[68:71], s93 offen offset:3072 sc1 lds\n"
    "v_mfma_f32_16x16x32_bf16 a[28:31], v[68:71], a[252:255], a[28:31]\n"
    "s_add_u32 s93, s93, 0x8000\n"
    "v_exp_f32_e32 v109, v109\n"
    "v_mfma_f32_16x16x32_bf16 a[32:35], v[72:75], a[224:227], a[32:35]\n"
    "s_add_u32 m0, s92, 0x1c000\n"
    "ds_read_b128 a[196:199], v17 offset:32768\n"
    "v_cvt_pk_bf16_f32 v106, v108, v109\n"
    "v_mfma_f32_16x16x32_bf16 a[36:39], v[72:75], a[228:231], a[36:39]\n"
    "v_exp_f32_e32 v110, v110\n"
    "v_mfma_f32_16x16x32_bf16 a[40:43], v[72:75], a[232:235], a[40:43]\n"
    "buffer_load_dwordx4 v22, s[68:71], s93 offen offset:0 sc1 lds\n"
    "v_mfma_f32_16x16x32_bf16 a[44:47], v[72:75], a[236:239], a[44:47]\n"
    "v_exp_f32_e32 v111, v111\n"
    "v_mfma_f32_16x16x32_bf16 a[48:51], v[72:75], a[240:243], a[48:51]\n"
    "ds_read_b128 a[200:203], v18 offset:32768\n"
    "v_cvt_pk_bf16_f32 v107, v110, v111\n"
    "v_mfma_f32_16x16x32_bf16 a[52:55], v[72:75], a[244:247], a[52:55]\n"
    "v_exp_f32_e32 v119, v119\n"
    "v_mfma_f32_16x16x32_bf16 a[56:59], v[72:75], a[248:251], a[56:59]\n"
    "buffer_load_dwordx4 v22, s[68:71], s93 offen offset:1024 sc1 lds\n"
    "v_mfma_f32_16x16x32_bf16 a[60:63], v[72:75], a[252:255], a[60:63]\n"
    "v_exp_f32_e32 v118, v118\n"
    "v_smfmac_f32_16x16x64_bf16 v[232:235], v[224:227], v[68:75], v228\n"
    "ds_read_b128 a[204:207], v19 offset:32768\n"
    "v_cvt_pk_bf16_f32 v119, v118, v119\n"
    "v_mfma_f32_16x16x32_bf16 a[64:67], v[84:87], a[224:227], a[64:67]\n"
    "buffer_load_dwordx4 v22, s[68:71], s93 offen offset:2048 sc1 lds\n"
    "v_mfma_f32_16x16x32_bf16 a[68:71], v[84:87], a[228:231], a[68:71]\n"
    "v_exp_f32_e32 v117, v117\n"
    "v_mfma_f32_16x16x32_bf16 a[72:75], v[84:87], a[232:235], a[72:75]\n"
    "buffer_load_dwordx4 v22, s[68:71], s93 offen offset:3072 sc1 lds\n"
    "v_mfma_f32_16x16x32_bf16 a[76:79], v[84:87], a[236:239], a[76:79]\n"
    "v_exp_f32_e32 v116, v116\n"
    "v_mfma_f32_16x16x32_bf16 a[80:83], v[84:87], a[240:243], a[80:83]\n"
    "ds_read_b128 a[208:211], v16 offset:36864\n"
    "v_cvt_pk_bf16_f32 v118, v116, v117\n"
    "v_mfma_f32_16x16x32_bf16 a[84:87], v[84:87], a[244:247], a[84:87]\n"
    "v_exp_f32_e32 v115, v115\n"
    "v_mfma_f32_16x16x32_bf16 a[88:91], v[84:87], a[248:251], a[88:91]\n"
    "ds_read_b128 a[212:215], v17 offset:36864\n"
    "v_mfma_f32_16x16x32_bf16 a[92:95], v[84:87], a[252:255], a[92:95]\n"
    "v_exp_f32_e32 v114, v114\n"
    "v_mfma_f32_16x16x32_bf16 a[96:99], v[88:91], a[224:227], a[96:99]\n"
    "ds_read_b128 a[216:219], v18 offset:36864\n"
    "v_cvt_pk_bf16_f32 v117, v114, v115\n"
    "v_mfma_f32_16x16x32_bf16 a[100:103], v[88:91], a[228:231], a[100:103]\n"
    "v_exp_f32_e32 v113, v113\n"
    "v_mfma_f32_16x16x32_bf16 a[104:107], v[88:91], a[232:235], a[104:107]\n"
    "ds_read_b128 a[220:223], v19 offset:36864\n"
    "v_mfma_f32_16x16x32_bf16 a[108:111], v[88:91], a[236:239], a[108:111]\n"
    "v_exp_f32_e32 v112, v112\n"
    "v_mfma_f32_16x16x32_bf16 a[112:115], v[88:91], a[240:243], a[112:115]\n"
    "v_cvt_pk_bf16_f32 v116, v112, v113\n"
    "v_mfma_f32_16x16x32_bf16 a[116:119], v[88:91], a[244:247], a[116:119]\n"
    "v_exp_f32_e32 v120, v120\n"
    "v_mfma_f32_16x16x32_bf16 a[120:123], v[88:91], a[248:251], a[120:123]\n"
    "v_mfma_f32_16x16x32_bf16 a[124:127], v[88:91], a[252:255], a[124:127]\n"
    "v_exp_f32_e32 v121, v121\n"
    "v_smfmac_f32_16x16x64_bf16 v[236:239], v[224:227], v[84:91], v228\n"
    "s_waitcnt lgkmcnt(1)\n"
    "v_cvt_pk_bf16_f32 v120, v120, v121\n"
    "v_mfma_f32_16x16x32_bf16 v[64:67], a[192:195], a[128:131], v[44:47]\n"
    "v_exp_f32_e32 v122, v122\n"
    "v_mfma_f32_16x16x32_bf16 v[64:67], a[196:199], a[132:135], v[64:67]\n"
    "ds_read_b128 a[224:227], v200 offset:24576\n"
    "v_mfma_f32_16x16x32_bf16 v[64:67], a[200:203], a[136:139], v[64:67]\n"
    "v_exp_f32_e32 v123, v123\n"
    "v_mfma_f32_16x16x32_bf16 v[64:67], a[204:207], a[140:143], v[64:67]\n"
    "ds_read_b128 a[228:231], v201 offset:24576\n"
    "v_cvt_pk_bf16_f32 v121, v122, v123\n"
    "v_mfma_f32_16x16x32_bf16 v[68:71], a[208:211], a[128:131], v[44:47]\n"
    "v_exp_f32_e32 v124, v124\n"
    "v_mfma_f32_16x16x32_bf16 v[68:71], a[212:215], a[132:135], v[68:71]\n"
    "ds_read_b128 a[232:235], v202 offset:24576\n"
    "v_mfma_f32_16x16x32_bf16 v[68:71], a[216:219], a[136:139], v[68:71]\n"
    "s_waitcnt lgkmcnt(3)\n"
    "v_exp_f32_e32 v125, v125\n"
    "v_mfma_f32_16x16x32_bf16 v[68:71], a[220:223], a[140:143], v[68:71]\n"
    "ds_read_b128 a[236:239], v203 offset:24576\n"
    "v_cvt_pk_bf16_f32 v122, v124, v125\n"
    "v_mfma_f32_16x16x32_bf16 v[72:75], a[192:195], a[144:147], v[48:51]\n"
    "v_exp_f32_e32 v126, v126\n"
    "v_mfma_f32_16x16x32_bf16 v[72:75], a[196:199], a[148:151], v[72:75]\n"
    "ds_read_b128 a[240:243], v204 offset:24576\n"
    "v_mfma_f32_16x16x32_bf16 v[72:75], a[200:203], a[152:155], v[72:75]\n"
    "v_exp_f32_e32 v127, v127\n"
    "v_mfma_f32_16x16x32_bf16 v[72:75], a[204:207], a[156:159], v[72:75]\n"
    "ds_read_b128 a[244:247], v205 offset:24576\n"
    "v_cvt_pk_bf16_f32 v123, v126, v127\n"
    "v_mfma_f32_16x16x32_bf16 v[76:79], a[208:211], a[144:147], v[48:51]\n"
    "v_exp_f32_e32 v71, v71\n"
    "v_mfma_f32_16x16x32_bf16 v[76:79], a[212:215], a[148:151], v[76:79]\n"
    "ds_read_b128 a[248:251], v206 offset:24576\n"
    "v_mfma_f32_16x16x32_bf16 v[76:79], a[216:219], a[152:155], v[76:79]\n"
    "v_exp_f32_e32 v70, v70\n"
    "v_mfma_f32_16x16x32_bf16 v[76:79], a[220:223], a[156:159], v[76:79]\n"
    "ds_read_b128 a[252:255], v207 offset:24576\n"
    "v_cvt_pk_bf16_f32 v71, v70, v71\n"
    "v_mfma_f32_16x16x32_bf16 v[80:83], a[192:195], a[160:163], v[52:55]\n"
    "v_mfma_f32_16x16x32_bf16 v[80:83], a[196:199], a[164:167], v[80:83]\n"
    "v_exp_f32_e32 v69, v69\n"
    "v_mfma_f32_16x16x32_bf16 v[80:83], a[200:203], a[168:171], v[80:83]\n"
    "v_mfma_f32_16x16x32_bf16 v[80:83], a[204:207], a[172:175], v[80:83]\n"
    "v_exp_f32_e32 v68, v68\n"
    "v_mfma_f32_16x16x32_bf16 v[84:87], a[208:211], a[160:163], v[52:55]\n"
    "v_cvt_pk_bf16_f32 v70, v68, v69\n"
    "v_mfma_f32_16x16x32_bf16 v[84:87], a[212:215], a[164:167], v[84:87]\n"
    "v_exp_f32_e32 v67, v67\n"
    "v_mfma_f32_16x16x32_bf16 v[84:87], a[216:219], a[168:171], v[84:87]\n"
    "v_mfma_f32_16x16x32_bf16 v[84:87], a[220:223], a[172:175], v[84:87]\n"
    "v_exp_f32_e32 v66, v66\n"
    "v_mfma_f32_16x16x32_bf16 v[88:91], a[192:195], a[176:179], v[56:59]\n"
    "v_cvt_pk_bf16_f32 v69, v66, v67\n"
    "v_mfma_f32_16x16x32_bf16 v[88:91], a[196:199], a[180:183], v[88:91]\n"
    "v_exp_f32_e32 v65, v65\n"
    "v_mfma_f32_16x16x32_bf16 v[88:91], a[200:203], a[184:187], v[88:91]\n"
    "v_mfma_f32_16x16x32_bf16 v[88:91], a[204:207], a[188:191], v[88:91]\n"
    "v_exp_f32_e32 v64, v64\n"
    "v_mfma_f32_16x16x32_bf16 v[92:95], a[208:211], a[176:179], v[56:59]\n"
    "v_cvt_pk_bf16_f32 v68, v64, v65\n"
    "v_mfma_f32_16x16x32_bf16 v[92:95], a[212:215], a[180:183], v[92:95]\n"
    "v_exp_f32_e32 v72, v72\n"
    "v_mfma_f32_16x16x32_bf16 v[92:95], a[216:219], a[184:187], v[92:95]\n"
    "v_mfma_f32_16x16x32_bf16 v[92:95], a[220:223], a[188:191], v[92:95]\n"
    "s_waitcnt lgkmcnt(0)\n"
    "v_exp_f32_e32 v73, v73\n"
    "v_mfma_f32_16x16x32_bf16 a[0:3], v[100:103], a[224:227], a[0:3]\n"
    "v_cvt_pk_bf16_f32 v72, v72, v73\n"
    "v_mfma_f32_16x16x32_bf16 a[4:7], v[100:103], a[228:231], a[4:7]\n"
    "v_exp_f32_e32 v74, v74\n"
    "v_mfma_f32_16x16x32_bf16 a[8:11], v[100:103], a[232:235], a[8:11]\n"
    "ds_read_b128 a[192:195], v16 offset:40960\n"
    "v_mfma_f32_16x16x32_bf16 a[12:15], v[100:103], a[236:239], a[12:15]\n"
    "v_exp_f32_e32 v75, v75\n"
    "v_mfma_f32_16x16x32_bf16 a[16:19], v[100:103], a[240:243], a[16:19]\n"
    "ds_read_b128 a[196:199], v17 offset:40960\n"
    "v_cvt_pk_bf16_f32 v73, v74, v75\n"
    "v_mfma_f32_16x16x32_bf16 a[20:23], v[100:103], a[244:247], a[20:23]\n"
    "v_exp_f32_e32 v76, v76\n"
    "v_mfma_f32_16x16x32_bf16 a[24:27], v[100:103], a[248:251], a[24:27]\n"
    "ds_read_b128 a[200:203], v18 offset:40960\n"
    "v_mfma_f32_16x16x32_bf16 a[28:31], v[100:103], a[252:255], a[28:31]\n"
    "v_exp_f32_e32 v77, v77\n"
    "v_mfma_f32_16x16x32_bf16 a[32:35], v[104:107], a[224:227], a[32:35]\n"
    "ds_read_b128 a[204:207], v19 offset:40960\n"
    "v_cvt_pk_bf16_f32 v74, v76, v77\n"
    "v_mfma_f32_16x16x32_bf16 a[36:39], v[104:107], a[228:231], a[36:39]\n"
    "v_exp_f32_e32 v78, v78\n"
    "v_mfma_f32_16x16x32_bf16 a[40:43], v[104:107], a[232:235], a[40:43]\n"
    "ds_read_b128 a[208:211], v16 offset:45056\n"
    "v_mfma_f32_16x16x32_bf16 a[44:47], v[104:107], a[236:239], a[44:47]\n"
    "v_exp_f32_e32 v79, v79\n"
    "v_mfma_f32_16x16x32_bf16 a[48:51], v[104:107], a[240:243], a[48:51]\n"
    "ds_read_b128 a[212:215], v17 offset:45056\n"
    "v_cvt_pk_bf16_f32 v75, v78, v79\n"
    "v_mfma_f32_16x16x32_bf16 a[52:55], v[104:107], a[244:247], a[52:55]\n"
    "v_exp_f32_e32 v87, v87\n"
    "v_mfma_f32_16x16x32_bf16 a[56:59], v[104:107], a[248:251], a[56:59]\n"
    "ds_read_b128 a[216:219], v18 offset:45056\n"
    "v_mfma_f32_16x16x32_bf16 a[60:63], v[104:107], a[252:255], a[60:63]\n"
    "v_exp_f32_e32 v86, v86\n"
    "v_smfmac_f32_16x16x64_bf16 v[232:235], v[224:227], v[100:107], v228\n"
    "ds_read_b128 a[220:223], v19 offset:45056\n"
    "v_cvt_pk_bf16_f32 v87, v86, v87\n"
    "v_mfma_f32_16x16x32_bf16 a[64:67], v[116:119], a[224:227], a[64:67]\n"
    "v_mfma_f32_16x16x32_bf16 a[68:71], v[116:119], a[228:231], a[68:71]\n"
    "v_exp_f32_e32 v85, v85\n"
    "v_mfma_f32_16x16x32_bf16 a[72:75], v[116:119], a[232:235], a[72:75]\n"
    "v_mfma_f32_16x16x32_bf16 a[76:79], v[116:119], a[236:239], a[76:79]\n"
    "v_exp_f32_e32 v84, v84\n"
    "v_mfma_f32_16x16x32_bf16 a[80:83], v[116:119], a[240:243], a[80:83]\n"
    "v_cvt_pk_bf16_f32 v86, v84, v85\n"
    "v_mfma_f32_16x16x32_bf16 a[84:87], v[116:119], a[244:247], a[84:87]\n"
    "v_exp_f32_e32 v83, v83\n"
    "v_mfma_f32_16x16x32_bf16 a[88:91], v[116:119], a[248:251], a[88:91]\n"
    "v_mfma_f32_16x16x32_bf16 a[92:95], v[116:119], a[252:255], a[92:95]\n"
    "v_exp_f32_e32 v82, v82\n"
    "v_mfma_f32_16x16x32_bf16 a[96:99], v[120:123], a[224:227], a[96:99]\n"
    "v_cvt_pk_bf16_f32 v85, v82, v83\n"
    "v_mfma_f32_16x16x32_bf16 a[100:103], v[120:123], a[228:231], a[100:103]\n"
    "v_exp_f32_e32 v81, v81\n"
    "v_mfma_f32_16x16x32_bf16 a[104:107], v[120:123], a[232:235], a[104:107]\n"
    "v_mfma_f32_16x16x32_bf16 a[108:111], v[120:123], a[236:239], a[108:111]\n"
    "v_exp_f32_e32 v80, v80\n"
    "v_mfma_f32_16x16x32_bf16 a[112:115], v[120:123], a[240:243], a[112:115]\n"
    "v_cvt_pk_bf16_f32 v84, v80, v81\n"
    "v_mfma_f32_16x16x32_bf16 a[116:119], v[120:123], a[244:247], a[116:119]\n"
    "v_exp_f32_e32 v88, v88\n"
    "v_mfma_f32_16x16x32_bf16 a[120:123], v[120:123], a[248:251], a[120:123]\n"
    "s_waitcnt vmcnt(0)\n"
    "v_mfma_f32_16x16x32_bf16 a[124:127], v[120:123], a[252:255], a[124:127]\n"
    "v_exp_f32_e32 v89, v89\n"
    "v_smfmac_f32_16x16x64_bf16 v[236:239], v[224:227], v[116:123], v228\n"
    "s_waitcnt lgkmcnt(0)\n"
    "v_cvt_pk_bf16_f32 v88, v88, v89\n"
    "v_mfma_f32_16x16x32_bf16 v[96:99], a[192:195], a[128:131], v[44:47]\n"
    "s_barrier\n"
    "v_exp_f32_e32 v90, v90\n"
    "v_mfma_f32_16x16x32_bf16 v[96:99], a[196:199], a[132:135], v[96:99]\n"
    "ds_read_b128 a[224:227], v200 offset:32768\n"
    "v_mfma_f32_16x16x32_bf16 v[96:99], a[200:203], a[136:139], v[96:99]\n"
    "v_exp_f32_e32 v91, v91\n"
    "v_mfma_f32_16x16x32_bf16 v[96:99], a[204:207], a[140:143], v[96:99]\n"
    "ds_read_b128 a[228:231], v201 offset:32768\n"
    "v_cvt_pk_bf16_f32 v89, v90, v91\n"
    "v_mfma_f32_16x16x32_bf16 v[100:103], a[208:211], a[128:131], v[44:47]\n"
    "v_exp_f32_e32 v92, v92\n"
    "v_mfma_f32_16x16x32_bf16 v[100:103], a[212:215], a[132:135], v[100:103]\n"
    "ds_read_b128 a[232:235], v202 offset:32768\n"
    "v_add_f32_e32 v232, v232, v233\n"
    "v_mfma_f32_16x16x32_bf16 v[100:103], a[216:219], a[136:139], v[100:103]\n"
    "v_exp_f32_e32 v93, v93\n"
    "v_add_f32_e32 v234, v234, v235\n"
    "v_mfma_f32_16x16x32_bf16 v[100:103], a[220:223], a[140:143], v[100:103]\n"
    "ds_read_b128 a[236:239], v203 offset:32768\n"
    "v_cvt_pk_bf16_f32 v90, v92, v93\n"
    "v_add_f32_e32 v236, v236, v237\n"
    "v_mfma_f32_16x16x32_bf16 v[104:107], a[192:195], a[144:147], v[48:51]\n"
    "v_exp_f32_e32 v94, v94\n"
    "v_add_f32_e32 v238, v238, v239\n"
    "v_mfma_f32_16x16x32_bf16 v[104:107], a[196:199], a[148:151], v[104:107]\n"
    "ds_read_b128 a[240:243], v204 offset:32768\n"
    "v_lshrrev_b32_e32 v13, 2, v135\n"
    "v_mfma_f32_16x16x32_bf16 v[104:107], a[200:203], a[152:155], v[104:107]\n"
    "v_exp_f32_e32 v95, v95\n"
    "v_and_b32_e32 v13, 0xfc, v13\n"
    "v_mfma_f32_16x16x32_bf16 v[104:107], a[204:207], a[156:159], v[104:107]\n"
    "ds_read_b128 a[244:247], v205 offset:32768\n"
    "v_cvt_pk_bf16_f32 v91, v94, v95\n"
    "v_and_b32_e32 v14, 0xfffffc00, v135\n"
    "v_mfma_f32_16x16x32_bf16 v[108:111], a[208:211], a[144:147], v[48:51]\n"
    "v_exp_f32_e32 v103, v103\n"
    "v_add_u32_e32 v13, v13, v14\n"
    "v_mfma_f32_16x16x32_bf16 v[108:111], a[212:215], a[148:151], v[108:111]\n"
    "ds_read_b128 a[248:251], v206 offset:32768\n"
    "v_and_b32_e32 v15, 0x30, v4\n"
    "v_mfma_f32_16x16x32_bf16 v[108:111], a[216:219], a[152:155], v[108:111]\n"
    "v_exp_f32_e32 v102, v102\n"
    "v_add_u32_e32 v14, v14, v15\n"
    "v_mfma_f32_16x16x32_bf16 v[108:111], a[220:223], a[156:159], v[108:111]\n"
    "ds_read_b128 a[252:255], v207 offset:32768\n"
    "v_cvt_pk_bf16_f32 v103, v102, v103\n"
    "v_mfma_f32_16x16x32_bf16 v[112:115], a[192:195], a[160:163], v[52:55]\n"
    "ds_write_b32 v13, v232 offset:0\n"
    "v_exp_f32_e32 v101, v101\n"
    "v_mfma_f32_16x16x32_bf16 v[112:115], a[196:199], a[164:167], v[112:115]\n"
    "ds_write_b32 v13, v234 offset:256\n"
    "v_mfma_f32_16x16x32_bf16 v[112:115], a[200:203], a[168:171], v[112:115]\n"
    "ds_write_b32 v13, v236 offset:512\n"
    "v_exp_f32_e32 v100, v100\n"
    "v_mfma_f32_16x16x32_bf16 v[112:115], a[204:207], a[172:175], v[112:115]\n"
    "ds_write_b32 v13, v238 offset:768\n"
    "v_cvt_pk_bf16_f32 v102, v100, v101\n"
    "v_mfma_f32_16x16x32_bf16 v[116:119], a[208:211], a[160:163], v[52:55]\n"
    "ds_read_b128 v[28:31], v14 offset:0\n"
    "v_exp_f32_e32 v99, v99\n"
    "v_mfma_f32_16x16x32_bf16 v[116:119], a[212:215], a[164:167], v[116:119]\n"
    "ds_read_b128 v[32:35], v14 offset:256\n"
    "v_mfma_f32_16x16x32_bf16 v[116:119], a[216:219], a[168:171], v[116:119]\n"
    "ds_read_b128 v[36:39], v14 offset:512\n"
    "v_exp_f32_e32 v98, v98\n"
    "v_mfma_f32_16x16x32_bf16 v[116:119], a[220:223], a[172:175], v[116:119]\n"
    "ds_read_b128 v[40:43], v14 offset:768\n"
    "v_cvt_pk_bf16_f32 v101, v98, v99\n"
    "v_mfma_f32_16x16x32_bf16 v[120:123], a[192:195], a[176:179], v[56:59]\n"
    "v_exp_f32_e32 v97, v97\n"
    "v_mfma_f32_16x16x32_bf16 v[120:123], a[196:199], a[180:183], v[120:123]\n"
    "v_mfma_f32_16x16x32_bf16 v[120:123], a[200:203], a[184:187], v[120:123]\n"
    "v_exp_f32_e32 v96, v96\n"
    "v_mfma_f32_16x16x32_bf16 v[120:123], a[204:207], a[188:191], v[120:123]\n"
    "v_cvt_pk_bf16_f32 v100, v96, v97\n"
    "v_mfma_f32_16x16x32_bf16 v[124:127], a[208:211], a[176:179], v[56:59]\n"
    "v_exp_f32_e32 v104, v104\n"
    "v_mfma_f32_16x16x32_bf16 v[124:127], a[212:215], a[180:183], v[124:127]\n"
    "v_mfma_f32_16x16x32_bf16 v[124:127], a[216:219], a[184:187], v[124:127]\n"
    "v_exp_f32_e32 v105, v105\n"
    "v_mfma_f32_16x16x32_bf16 v[124:127], a[220:223], a[188:191], v[124:127]\n"
    "v_cvt_pk_bf16_f32 v104, v104, v105\n"
    "s_waitcnt lgkmcnt(0)\n"
    "v_mfma_f32_16x16x32_bf16 a[0:3], v[68:71], a[224:227], a[0:3]\n"
    "v_exp_f32_e32 v106, v106\n"
    "v_mfma_f32_16x16x32_bf16 a[4:7], v[68:71], a[228:231], a[4:7]\n"
    "ds_read_b128 a[192:195], v16 offset:49152\n"
    "v_mfma_f32_16x16x32_bf16 a[8:11], v[68:71], a[232:235], a[8:11]\n"
    "v_exp_f32_e32 v107, v107\n"
    "v_mfma_f32_16x16x32_bf16 a[12:15], v[68:71], a[236:239], a[12:15]\n"
    "ds_read_b128 a[196:199], v17 offset:49152\n"
    "v_cvt_pk_bf16_f32 v105, v106, v107\n"
    "v_mfma_f32_16x16x32_bf16 a[16:19], v[68:71], a[240:243], a[16:19]\n"
    "v_exp_f32_e32 v108, v108\n"
    "v_mfma_f32_16x16x32_bf16 a[20:23], v[68:71], a[244:247], a[20:23]\n"
    "ds_read_b128 a[200:203], v18 offset:49152\n"
    "v_mfma_f32_16x16x32_bf16 a[24:27], v[68:71], a[248:251], a[24:27]\n"
    "v_exp_f32_e32 v109, v109\n"
    "v_mfma_f32_16x16x32_bf16 a[28:31], v[68:71], a[252:255], a[28:31]\n"
    "ds_read_b128 a[204:207], v19 offset:49152\n"
    "v_cvt_pk_bf16_f32 v106, v108, v109\n"
    "v_mfma_f32_16x16x32_bf16 v[28:31], v[68:71], v[24:27], v[28:31]\n"
    "v_exp_f32_e32 v110, v110\n"
    "v_mfma_f32_16x16x32_bf16 a[32:35], v[72:75], a[224:227], a[32:35]\n"
    "ds_read_b128 a[208:211], v16 offset:53248\n"
    "v_mfma_f32_16x16x32_bf16 a[36:39], v[72:75], a[228:231], a[36:39]\n"
    "v_exp_f32_e32 v111, v111\n"
    "v_mfma_f32_16x16x32_bf16 a[40:43], v[72:75], a[232:235], a[40:43]\n"
    "ds_read_b128 a[212:215], v17 offset:53248\n"
    "v_cvt_pk_bf16_f32 v107, v110, v111\n"
    "v_mfma_f32_16x16x32_bf16 a[44:47], v[72:75], a[236:239], a[44:47]\n"
    "v_exp_f32_e32 v119, v119\n"
    "v_mfma_f32_16x16x32_bf16 a[48:51], v[72:75], a[240:243], a[48:51]\n"
    "ds_read_b128 a[216:219], v18 offset:53248\n"
    "v_mfma_f32_16x16x32_bf16 a[52:55], v[72:75], a[244:247], a[52:55]\n"
    "v_exp_f32_e32 v118, v118\n"
    "v_mfma_f32_16x16x32_bf16 a[56:59], v[72:75], a[248:251], a[56:59]\n"
    "ds_read_b128 a[220:223], v19 offset:53248\n"
    "v_cvt_pk_bf16_f32 v119, v118, v119\n"
    "v_mfma_f32_16x16x32_bf16 a[60:63], v[72:75], a[252:255], a[60:63]\n"
    "v_exp_f32_e32 v117, v117\n"
    "v_mfma_f32_16x16x32_bf16 v[32:35], v[72:75], v[24:27], v[32:35]\n"
    "ds_read_b128 v[128:131], v200 offset:40960\n"
    "v_mfma_f32_16x16x32_bf16 a[64:67], v[84:87], a[224:227], a[64:67]\n"
    "v_exp_f32_e32 v116, v116\n"
    "v_mfma_f32_16x16x32_bf16 a[68:71], v[84:87], a[228:231], a[68:71]\n"
    "ds_read_b128 v[132:135], v201 offset:40960\n"
    "v_cvt_pk_bf16_f32 v118, v116, v117\n"
    "v_mfma_f32_16x16x32_bf16 a[72:75], v[84:87], a[232:235], a[72:75]\n"
    "v_exp_f32_e32 v115, v115\n"
    "v_mfma_f32_16x16x32_bf16 a[76:79], v[84:87], a[236:239], a[76:79]\n"
    "ds_read_b128 v[136:139], v202 offset:40960\n"
    "v_mfma_f32_16x16x32_bf16 a[80:83], v[84:87], a[240:243], a[80:83]\n"
    "v_exp_f32_e32 v114, v114\n"
    "v_mfma_f32_16x16x32_bf16 a[84:87], v[84:87], a[244:247], a[84:87]\n"
    "ds_read_b128 v[140:143], v203 offset:40960\n"
    "v_cvt_pk_bf16_f32 v117, v114, v115\n"
    "v_mfma_f32_16x16x32_bf16 a[88:91], v[84:87], a[248:251], a[88:91]\n"
    "v_exp_f32_e32 v113, v113\n"
    "v_mfma_f32_16x16x32_bf16 a[92:95], v[84:87], a[252:255], a[92:95]\n"
    "ds_read_b128 v[144:147], v204 offset:40960\n"
    "v_mfma_f32_16x16x32_bf16 v[36:39], v[84:87], v[24:27], v[36:39]\n"
    "v_exp_f32_e32 v112, v112\n"
    "v_mfma_f32_16x16x32_bf16 a[96:99], v[88:91], a[224:227], a[96:99]\n"
    "ds_read_b128 v[148:151], v205 offset:40960\n"
    "v_cvt_pk_bf16_f32 v116, v112, v113\n"
    "v_mfma_f32_16x16x32_bf16 a[100:103], v[88:91], a[228:231], a[100:103]\n"
    "v_exp_f32_e32 v120, v120\n"
    "v_mfma_f32_16x16x32_bf16 a[104:107], v[88:91], a[232:235], a[104:107]\n"
    "ds_read_b128 v[152:155], v206 offset:40960\n"
    "v_mfma_f32_16x16x32_bf16 a[108:111], v[88:91], a[236:239], a[108:111]\n"
    "v_exp_f32_e32 v121, v121\n"
    "v_mfma_f32_16x16x32_bf16 a[112:115], v[88:91], a[240:243], a[112:115]\n"
    "ds_read_b128 v[156:159], v207 offset:40960\n"
    "v_cvt_pk_bf16_f32 v120, v120, v121\n"
    "v_mfma_f32_16x16x32_bf16 a[116:119], v[88:91], a[244:247], a[116:119]\n"
    "v_exp_f32_e32 v122, v122\n"
    "v_mfma_f32_16x16x32_bf16 a[120:123], v[88:91], a[248:251], a[120:123]\n"
    "ds_read_b128 v[160:163], v16 offset:57344\n"
    "v_mfma_f32_16x16x32_bf16 a[124:127], v[88:91], a[252:255], a[124:127]\n"
    "v_exp_f32_e32 v123, v123\n"
    "v_mfma_f32_16x16x32_bf16 v[40:43], v[88:91], v[24:27], v[40:43]\n"
    "ds_read_b128 v[164:167], v17 offset:57344\n"
    "v_cvt_pk_bf16_f32 v121, v122, v123\n"
    "s_waitcnt lgkmcnt(10)\n"
    "v_mfma_f32_16x16x32_bf16 v[64:67], a[192:195], a[128:131], v[44:47]\n"
    "v_exp_f32_e32 v124, v124\n"
    "v_mfma_f32_16x16x32_bf16 v[64:67], a[196:199], a[132:135], v[64:67]\n"
    "ds_read_b128 v[168:171], v18 offset:57344\n"
    "v_mfma_f32_16x16x32_bf16 v[64:67], a[200:203], a[136:139], v[64:67]\n"
    "v_exp_f32_e32 v125, v125\n"
    "v_mfma_f32_16x16x32_bf16 v[64:67], a[204:207], a[140:143], v[64:67]\n"
    "ds_read_b128 v[172:175], v19 offset:57344\n"
    "v_cvt_pk_bf16_f32 v122, v124, v125\n"
    "v_mfma_f32_16x16x32_bf16 v[68:71], a[208:211], a[128:131], v[44:47]\n"
    "v_exp_f32_e32 v126, v126\n"
    "v_mfma_f32_16x16x32_bf16 v[68:71], a[212:215], a[132:135], v[68:71]\n"
    "ds_read_b128 v[176:179], v16 offset:61440\n"
    "v_mfma_f32_16x16x32_bf16 v[68:71], a[216:219], a[136:139], v[68:71]\n"
    "v_exp_f32_e32 v127, v127\n"
    "v_mfma_f32_16x16x32_bf16 v[68:71], a[220:223], a[140:143], v[68:71]\n"
    "ds_read_b128 v[180:183], v17 offset:61440\n"
    "v_cvt_pk_bf16_f32 v123, v126, v127\n"
    "s_waitcnt lgkmcnt(6)\n"
    "v_mfma_f32_16x16x32_bf16 a[0:3], v[100:103], v[128:131], a[0:3]\n"
    "ds_read_b128 v[184:187], v18 offset:61440\n"
    "v_mfma_f32_16x16x32_bf16 a[4:7], v[100:103], v[132:135], a[4:7]\n"
    "ds_read_b128 v[188:191], v19 offset:61440\n"
    "v_mfma_f32_16x16x32_bf16 a[8:11], v[100:103], v[136:139], a[8:11]\n"
    "ds_read_b128 v[208:211], v200 offset:49152\n"
    "v_mfma_f32_16x16x32_bf16 a[12:15], v[100:103], v[140:143], a[12:15]\n"
    "ds_read_b128 v[212:215], v201 offset:49152\n"
    "v_mfma_f32_16x16x32_bf16 a[16:19], v[100:103], v[144:147], a[16:19]\n"
    "v_exp_f32_e32 v71, v71\n"
    "v_mfma_f32_16x16x32_bf16 a[20:23], v[100:103], v[148:151], a[20:23]\n"
    "ds_read_b128 v[216:219], v202 offset:49152\n"
    "v_mfma_f32_16x16x32_bf16 a[24:27], v[100:103], v[152:155], a[24:27]\n"
    "v_exp_f32_e32 v70, v70\n"
    "v_mfma_f32_16x16x32_bf16 a[28:31], v[100:103], v[156:159], a[28:31]\n"
    "ds_read_b128 v[220:223], v203 offset:49152\n"
    "v_cvt_pk_bf16_f32 v71, v70, v71\n"
    "v_mfma_f32_16x16x32_bf16 v[28:31], v[100:103], v[24:27], v[28:31]\n"
    "v_exp_f32_e32 v69, v69\n"
    "s_waitcnt lgkmcnt(4)\n"
    "v_mfma_f32_16x16x32_bf16 v[96:99], v[160:163], a[128:131], v[44:47]\n"
    "ds_read_b128 v[224:227], v204 offset:49152\n"
    "v_mfma_f32_16x16x32_bf16 v[96:99], v[164:167], a[132:135], v[96:99]\n"
    "v_exp_f32_e32 v68, v68\n"
    "v_mfma_f32_16x16x32_bf16 v[96:99], v[168:171], a[136:139], v[96:99]\n"
    "ds_read_b128 v[228:231], v205 offset:49152\n"
    "v_cvt_pk_bf16_f32 v70, v68, v69\n"
    "v_mfma_f32_16x16x32_bf16 v[96:99], v[172:175], a[140:143], v[96:99]\n"
    "v_exp_f32_e32 v67, v67\n"
    "v_mfma_f32_16x16x32_bf16 v[100:103], v[176:179], a[128:131], v[44:47]\n"
    "ds_read_b128 v[232:235], v206 offset:49152\n"
    "v_mfma_f32_16x16x32_bf16 v[100:103], v[180:183], a[132:135], v[100:103]\n"
    "v_exp_f32_e32 v66, v66\n"
    "v_mfma_f32_16x16x32_bf16 v[100:103], v[184:187], a[136:139], v[100:103]\n"
    "ds_read_b128 v[236:239], v207 offset:49152\n"
    "v_cvt_pk_bf16_f32 v69, v66, v67\n"
    "v_mfma_f32_16x16x32_bf16 v[100:103], v[188:191], a[140:143], v[100:103]\n"
    "v_exp_f32_e32 v65, v65\n"
    "v_mfma_f32_16x16x32_bf16 v[72:75], a[192:195], a[144:147], v[48:51]\n"
    "ds_read_b128 a[224:227], v200 offset:57344\n"
    "v_mfma_f32_16x16x32_bf16 v[72:75], a[196:199], a[148:151], v[72:75]\n"
    "v_exp_f32_e32 v64, v64\n"
    "v_mfma_f32_16x16x32_bf16 v[72:75], a[200:203], a[152:155], v[72:75]\n"
    "ds_read_b128 a[228:231], v201 offset:57344\n"
    "v_cvt_pk_bf16_f32 v68, v64, v65\n"
    "v_mfma_f32_16x16x32_bf16 v[72:75], a[204:207], a[156:159], v[72:75]\n"
    "ds_read_b128 a[232:235], v202 offset:57344\n"
    "v_mfma_f32_16x16x32_bf16 v[76:79], a[208:211], a[144:147], v[48:51]\n"
    "v_exp_f32_e32 v103, v103\n"
    "v_mfma_f32_16x16x32_bf16 v[76:79], a[212:215], a[148:151], v[76:79]\n"
    "ds_read_b128 a[236:239], v203 offset:57344\n"
    "v_mfma_f32_16x16x32_bf16 v[76:79], a[216:219], a[152:155], v[76:79]\n"
    "v_exp_f32_e32 v102, v102\n"
    "v_mfma_f32_16x16x32_bf16 v[76:79], a[220:223], a[156:159], v[76:79]\n"
    "ds_read_b128 a[240:243], v204 offset:57344\n"
    "v_cvt_pk_bf16_f32 v103, v102, v103\n"
    "v_mfma_f32_16x16x32_bf16 a[32:35], v[104:107], v[128:131], a[32:35]\n"
    "v_exp_f32_e32 v101, v101\n"
    "v_mfma_f32_16x16x32_bf16 a[36:39], v[104:107], v[132:135], a[36:39]\n"
    "ds_read_b128 a[244:247], v205 offset:57344\n"
    "v_mfma_f32_16x16x32_bf16 a[40:43], v[104:107], v[136:139], a[40:43]\n"
    "v_exp_f32_e32 v100, v100\n"
    "v_mfma_f32_16x16x32_bf16 a[44:47], v[104:107], v[140:143], a[44:47]\n"
    "ds_read_b128 a[248:251], v206 offset:57344\n"
    "v_cvt_pk_bf16_f32 v102, v100, v101\n"
    "v_mfma_f32_16x16x32_bf16 a[48:51], v[104:107], v[144:147], a[48:51]\n"
    "v_exp_f32_e32 v99, v99\n"
    "v_mfma_f32_16x16x32_bf16 a[52:55], v[104:107], v[148:151], a[52:55]\n"
    "ds_read_b128 a[252:255], v207 offset:57344\n"
    "v_mfma_f32_16x16x32_bf16 a[56:59], v[104:107], v[152:155], a[56:59]\n"
    "v_exp_f32_e32 v98, v98\n"
    "v_mfma_f32_16x16x32_bf16 a[60:63], v[104:107], v[156:159], a[60:63]\n"
    "v_cvt_pk_bf16_f32 v101, v98, v99\n"
    "v_mov_b32_e32 v12, 0x25000\n"
    "v_mfma_f32_16x16x32_bf16 v[32:35], v[104:107], v[24:27], v[32:35]\n"
    "v_exp_f32_e32 v97, v97\n"
    "v_mfma_f32_16x16x32_bf16 v[104:107], v[160:163], a[144:147], v[48:51]\n"
    "ds_read_b32 v12, v12\n"
    "v_mfma_f32_16x16x32_bf16 v[104:107], v[164:167], a[148:151], v[104:107]\n"
    "v_exp_f32_e32 v96, v96\n"
    "v_mfma_f32_16x16x32_bf16 v[104:107], v[168:171], a[152:155], v[104:107]\n"
    "v_cvt_pk_bf16_f32 v100, v96, v97\n"
    "v_and_b32_e32 v13, 15, v4\n"
    "v_mfma_f32_16x16x32_bf16 v[104:107], v[172:175], a[156:159], v[104:107]\n"
    "v_exp_f32_e32 v72, v72\n"
    "v_mfma_f32_16x16x32_bf16 v[108:111], v[176:179], a[144:147], v[48:51]\n"
    "v_lshrrev_b32_e32 v14, 4, v4\n"
    "v_mfma_f32_16x16x32_bf16 v[108:111], v[180:183], a[148:151], v[108:111]\n"
    "v_exp_f32_e32 v73, v73\n"
    "v_mfma_f32_16x16x32_bf16 v[108:111], v[184:187], a[152:155], v[108:111]\n"
    "v_cvt_pk_bf16_f32 v72, v72, v73\n"
    "v_lshlrev_b32_e32 v13, 4, v13\n"
    "v_mfma_f32_16x16x32_bf16 v[108:111], v[188:191], a[156:159], v[108:111]\n"
    "v_exp_f32_e32 v74, v74\n"
    "s_waitcnt lgkmcnt(8)\n"
    "v_mfma_f32_16x16x32_bf16 a[0:3], v[68:71], v[208:211], a[0:3]\n"
    "v_lshl_add_u32 v13, v14, 10, v13\n"
    "v_mfma_f32_16x16x32_bf16 a[4:7], v[68:71], v[212:215], a[4:7]\n"
    "v_exp_f32_e32 v75, v75\n"
    "v_mfma_f32_16x16x32_bf16 a[8:11], v[68:71], v[216:219], a[8:11]\n"
    "v_cvt_pk_bf16_f32 v73, v74, v75\n"
    "v_add_u32_e32 v5, 0x1000, v13\n"
    "v_mfma_f32_16x16x32_bf16 a[12:15], v[68:71], v[220:223], a[12:15]\n"
    "v_exp_f32_e32 v76, v76\n"
    "v_mfma_f32_16x16x32_bf16 a[16:19], v[68:71], v[224:227], a[16:19]\n"
    "v_add_u32_e32 v6, 0x3000, v13\n"
    "v_mfma_f32_16x16x32_bf16 a[20:23], v[68:71], v[228:231], a[20:23]\n"
    "v_exp_f32_e32 v77, v77\n"
    "v_mfma_f32_16x16x32_bf16 a[24:27], v[68:71], v[232:235], a[24:27]\n"
    "v_cvt_pk_bf16_f32 v74, v76, v77\n"
    "v_mfma_f32_16x16x32_bf16 a[28:31], v[68:71], v[236:239], a[28:31]\n"
    "v_exp_f32_e32 v78, v78\n"
    "v_mfma_f32_16x16x32_bf16 v[28:31], v[68:71], v[24:27], v[28:31]\n"
    "s_waitcnt lgkmcnt(0)\n"
    "v_mfma_f32_16x16x32_bf16 a[0:3], v[100:103], a[224:227], a[0:3]\n"
    "v_exp_f32_e32 v79, v79\n"
    "v_mfma_f32_16x16x32_bf16 a[4:7], v[100:103], a[228:231], a[4:7]\n"
    "v_cvt_pk_bf16_f32 v75, v78, v79\n"
    "v_mfma_f32_16x16x32_bf16 a[8:11], v[100:103], a[232:235], a[8:11]\n"
    "v_exp_f32_e32 v104, v104\n"
    "v_mfma_f32_16x16x32_bf16 a[12:15], v[100:103], a[236:239], a[12:15]\n"
    "v_mfma_f32_16x16x32_bf16 a[16:19], v[100:103], a[240:243], a[16:19]\n"
    "v_exp_f32_e32 v105, v105\n"
    "v_mfma_f32_16x16x32_bf16 a[20:23], v[100:103], a[244:247], a[20:23]\n"
    "v_cvt_pk_bf16_f32 v104, v104, v105\n"
    "v_mfma_f32_16x16x32_bf16 a[24:27], v[100:103], a[248:251], a[24:27]\n"
    "v_exp_f32_e32 v106, v106\n"
    "v_mfma_f32_16x16x32_bf16 a[28:31], v[100:103], a[252:255], a[28:31]\n"
    "v_mfma_f32_16x16x32_bf16 v[28:31], v[100:103], v[24:27], v[28:31]\n"
    "v_exp_f32_e32 v107, v107\n"
    "v_mfma_f32_16x16x32_bf16 v[80:83], a[192:195], a[160:163], v[52:55]\n"
    "v_cvt_pk_bf16_f32 v105, v106, v107\n"
    "v_mfma_f32_16x16x32_bf16 v[80:83], a[196:199], a[164:167], v[80:83]\n"
    "v_exp_f32_e32 v108, v108\n"
    "v_mfma_f32_16x16x32_bf16 v[80:83], a[200:203], a[168:171], v[80:83]\n"
    "v_mfma_f32_16x16x32_bf16 v[80:83], a[204:207], a[172:175], v[80:83]\n"
    "v_exp_f32_e32 v109, v109\n"
    "v_mfma_f32_16x16x32_bf16 v[84:87], a[208:211], a[160:163], v[52:55]\n"
    "v_cvt_pk_bf16_f32 v106, v108, v109\n"
    "v_mfma_f32_16x16x32_bf16 v[84:87], a[212:215], a[164:167], v[84:87]\n"
    "v_exp_f32_e32 v110, v110\n"
    "v_rcp_f32_e32 v8, v28\n"
    "v_mfma_f32_16x16x32_bf16 v[84:87], a[216:219], a[168:171], v[84:87]\n"
    "v_rcp_f32_e32 v9, v29\n"
    "v_mfma_f32_16x16x32_bf16 v[84:87], a[220:223], a[172:175], v[84:87]\n"
    "v_exp_f32_e32 v111, v111\n"
    "v_rcp_f32_e32 v10, v30\n"
    "v_mfma_f32_16x16x32_bf16 a[64:67], v[116:119], v[128:131], a[64:67]\n"
    "v_cvt_pk_bf16_f32 v107, v110, v111\n"
    "v_rcp_f32_e32 v11, v31\n"
    "v_mfma_f32_16x16x32_bf16 a[68:71], v[116:119], v[132:135], a[68:71]\n"
    "v_accvgpr_read_b32 v240, a0\n"
    "v_accvgpr_read_b32 v241, a4\n"
    "v_accvgpr_read_b32 v242, a8\n"
    "v_accvgpr_read_b32 v243, a12\n"
    "v_mfma_f32_16x16x32_bf16 a[72:75], v[116:119], v[136:139], a[72:75]\n"
    "v_accvgpr_read_b32 v244, a16\n"
    "v_accvgpr_read_b32 v245, a20\n"
    "v_accvgpr_read_b32 v246, a24\n"
    "v_accvgpr_read_b32 v247, a28\n"
    "v_mfma_f32_16x16x32_bf16 a[76:79], v[116:119], v[140:143], a[76:79]\n"
    "v_mul_f32_e32 v240, v240, v8\n"
    "v_mul_f32_e32 v241, v241, v8\n"
    "v_mul_f32_e32 v242, v242, v8\n"
    "v_mul_f32_e32 v243, v243, v8\n"
    "v_mfma_f32_16x16x32_bf16 a[80:83], v[116:119], v[144:147], a[80:83]\n"
    "v_exp_f32_e32 v87, v87\n"
    "v_mul_f32_e32 v244, v244, v8\n"
    "v_mul_f32_e32 v245, v245, v8\n"
    "v_mfma_f32_16x16x32_bf16 a[84:87], v[116:119], v[148:151], a[84:87]\n"
    "v_mul_f32_e32 v246, v246, v8\n"
    "v_mul_f32_e32 v247, v247, v8\n"
    "v_cvt_pk_bf16_f32 v192, v240, v241\n"
    "v_cvt_pk_bf16_f32 v193, v242, v243\n"
    "v_mfma_f32_16x16x32_bf16 a[88:91], v[116:119], v[152:155], a[88:91]\n"
    "v_exp_f32_e32 v86, v86\n"
    "v_cvt_pk_bf16_f32 v194, v244, v245\n"
    "v_cvt_pk_bf16_f32 v195, v246, v247\n"
    "v_mfma_f32_16x16x32_bf16 a[92:95], v[116:119], v[156:159], a[92:95]\n"
    "global_store_dwordx4 v5, v[192:195], s[62:63] offset:-4096 sc1\n"
    "v_cvt_pk_bf16_f32 v87, v86, v87\n"
    "v_mfma_f32_16x16x32_bf16 v[36:39], v[116:119], v[24:27], v[36:39]\n"
    "v_exp_f32_e32 v85, v85\n"
    "v_accvgpr_read_b32 v248, a1\n"
    "v_accvgpr_read_b32 v249, a5\n"
    "v_mfma_f32_16x16x32_bf16 v[112:115], v[160:163], a[160:163], v[52:55]\n"
    "v_accvgpr_read_b32 v250, a9\n"
    "v_accvgpr_read_b32 v251, a13\n"
    "v_accvgpr_read_b32 v252, a17\n"
    "v_accvgpr_read_b32 v253, a21\n"
    "v_mfma_f32_16x16x32_bf16 v[112:115], v[164:167], a[164:167], v[112:115]\n"
    "v_exp_f32_e32 v84, v84\n"
    "v_accvgpr_read_b32 v254, a25\n"
    "v_accvgpr_read_b32 v255, a29\n"
    "v_mfma_f32_16x16x32_bf16 v[112:115], v[168:171], a[168:171], v[112:115]\n"
    "v_cvt_pk_bf16_f32 v86, v84, v85\n"
    "v_mul_f32_e32 v248, v248, v9\n"
    "v_mul_f32_e32 v249, v249, v9\n"
    "v_mul_f32_e32 v250, v250, v9\n"
    "v_mfma_f32_16x16x32_bf16 v[112:115], v[172:175], a[172:175], v[112:115]\n"
    "v_exp_f32_e32 v83, v83\n"
    "v_mul_f32_e32 v251, v251, v9\n"
    "v_mul_f32_e32 v252, v252, v9\n"
    "v_mfma_f32_16x16x32_bf16 v[116:119], v[176:179], a[160:163], v[52:55]\n"
    "v_mul_f32_e32 v253, v253, v9\n"
    "v_mul_f32_e32 v254, v254, v9\n"
    "v_mul_f32_e32 v255, v255, v9\n"
    "v_cvt_pk_bf16_f32 v196, v248, v249\n"
    "v_mfma_f32_16x16x32_bf16 v[116:119], v[180:183], a[164:167], v[116:119]\n"
    "v_exp_f32_e32 v82, v82\n"
    "v_cvt_pk_bf16_f32 v197, v250, v251\n"
    "v_cvt_pk_bf16_f32 v198, v252, v253\n"
    "v_mfma_f32_16x16x32_bf16 v[116:119], v[184:187], a[168:171], v[116:119]\n"
    "v_cvt_pk_bf16_f32 v85, v82, v83\n"
    "v_cvt_pk_bf16_f32 v199, v254, v255\n"
    "v_mfma_f32_16x16x32_bf16 v[116:119], v[188:191], a[172:175], v[116:119]\n"
    "global_store_dwordx4 v5, v[196:199], s[62:63] offset:-3840 sc1\n"
    "v_exp_f32_e32 v81, v81\n"
    "v_mfma_f32_16x16x32_bf16 a[32:35], v[72:75], v[208:211], a[32:35]\n"
    "v_accvgpr_read_b32 v240, a2\n"
    "v_accvgpr_read_b32 v241, a6\n"
    "v_accvgpr_read_b32 v242, a10\n"
    "v_accvgpr_read_b32 v243, a14\n"
    "v_mfma_f32_16x16x32_bf16 a[36:39], v[72:75], v[212:215], a[36:39]\n"
    "v_exp_f32_e32 v80, v80\n"
    "v_accvgpr_read_b32 v244, a18\n"
    "v_accvgpr_read_b32 v245, a22\n"
    "v_mfma_f32_16x16x32_bf16 a[40:43], v[72:75], v[216:219], a[40:43]\n"
    "v_cvt_pk_bf16_f32 v84, v80, v81\n"
    "v_accvgpr_read_b32 v246, a26\n"
    "v_accvgpr_read_b32 v247, a30\n"
    "v_mul_f32_e32 v240, v240, v10\n"
    "v_mfma_f32_16x16x32_bf16 a[44:47], v[72:75], v[220:223], a[44:47]\n"
    "v_mul_f32_e32 v241, v241, v10\n"
    "v_mul_f32_e32 v242, v242, v10\n"
    "v_mul_f32_e32 v243, v243, v10\n"
    "v_mul_f32_e32 v244, v244, v10\n"
    "v_mfma_f32_16x16x32_bf16 a[48:51], v[72:75], v[224:227], a[48:51]\n"
    "v_exp_f32_e32 v119, v119\n"
    "v_mul_f32_e32 v245, v245, v10\n"
    "v_mul_f32_e32 v246, v246, v10\n"
    "v_mfma_f32_16x16x32_bf16 a[52:55], v[72:75], v[228:231], a[52:55]\n"
    "v_mul_f32_e32 v247, v247, v10\n"
    "v_cvt_pk_bf16_f32 v192, v240, v241\n"
    "v_cvt_pk_bf16_f32 v193, v242, v243\n"
    "v_cvt_pk_bf16_f32 v194, v244, v245\n"
    "v_mfma_f32_16x16x32_bf16 a[56:59], v[72:75], v[232:235], a[56:59]\n"
    "v_exp_f32_e32 v118, v118\n"
    "v_cvt_pk_bf16_f32 v195, v246, v247\n"
    "v_mfma_f32_16x16x32_bf16 a[60:63], v[72:75], v[236:239], a[60:63]\n"
    "global_store_dwordx4 v5, v[192:195], s[62:63] offset:-3584 sc1\n"
    "v_cvt_pk_bf16_f32 v119, v118, v119\n"
    "v_mfma_f32_16x16x32_bf16 v[32:35], v[72:75], v[24:27], v[32:35]\n"
    "v_exp_f32_e32 v117, v117\n"
    "v_accvgpr_read_b32 v248, a3\n"
    "v_accvgpr_read_b32 v249, a7\n"
    "v_mfma_f32_16x16x32_bf16 a[32:35], v[104:107], a[224:227], a[32:35]\n"
    "v_accvgpr_read_b32 v250, a11\n"
    "v_accvgpr_read_b32 v251, a15\n"
    "v_accvgpr_read_b32 v252, a19\n"
    "v_accvgpr_read_b32 v253, a23\n"
    "v_mfma_f32_16x16x32_bf16 a[36:39], v[104:107], a[228:231], a[36:39]\n"
    "v_exp_f32_e32 v116, v116\n"
    "v_accvgpr_read_b32 v254, a27\n"
    "v_accvgpr_read_b32 v255, a31\n"
    "v_mfma_f32_16x16x32_bf16 a[40:43], v[104:107], a[232:235], a[40:43]\n"
    "v_cvt_pk_bf16_f32 v118, v116, v117\n"
    "v_mul_f32_e32 v248, v248, v11\n"
    "v_mul_f32_e32 v249, v249, v11\n"
    "v_mul_f32_e32 v250, v250, v11\n"
    "v_mfma_f32_16x16x32_bf16 a[44:47], v[104:107], a[236:239], a[44:47]\n"
    "v_exp_f32_e32 v115, v115\n"
    "v_mul_f32_e32 v251, v251, v11\n"
    "v_mul_f32_e32 v252, v252, v11\n"
    "v_mfma_f32_16x16x32_bf16 a[48:51], v[104:107], a[240:243], a[48:51]\n"
    "v_mul_f32_e32 v253, v253, v11\n"
    "v_mul_f32_e32 v254, v254, v11\n"
    "v_mul_f32_e32 v255, v255, v11\n"
    "v_cvt_pk_bf16_f32 v196, v248, v249\n"
    "v_mfma_f32_16x16x32_bf16 a[52:55], v[104:107], a[244:247], a[52:55]\n"
    "v_exp_f32_e32 v114, v114\n"
    "v_cvt_pk_bf16_f32 v197, v250, v251\n"
    "v_cvt_pk_bf16_f32 v198, v252, v253\n"
    "v_mfma_f32_16x16x32_bf16 a[56:59], v[104:107], a[248:251], a[56:59]\n"
    "v_cvt_pk_bf16_f32 v117, v114, v115\n"
    "v_cvt_pk_bf16_f32 v199, v254, v255\n"
    "v_mfma_f32_16x16x32_bf16 a[60:63], v[104:107], a[252:255], a[60:63]\n"
    "global_store_dwordx4 v5, v[196:199], s[62:63] offset:-3328 sc1\n"
    "v_exp_f32_e32 v113, v113\n"
    "v_mfma_f32_16x16x32_bf16 v[32:35], v[104:107], v[24:27], v[32:35]\n"
    "v_mfma_f32_16x16x32_bf16 v[88:91], a[192:195], a[176:179], v[56:59]\n"
    "v_exp_f32_e32 v112, v112\n"
    "v_mfma_f32_16x16x32_bf16 v[88:91], a[196:199], a[180:183], v[88:91]\n"
    "v_cvt_pk_bf16_f32 v116, v112, v113\n"
    "v_mfma_f32_16x16x32_bf16 v[88:91], a[200:203], a[184:187], v[88:91]\n"
    "v_mfma_f32_16x16x32_bf16 v[88:91], a[204:207], a[188:191], v[88:91]\n"
    "v_mfma_f32_16x16x32_bf16 v[92:95], a[208:211], a[176:179], v[56:59]\n"
    "v_mfma_f32_16x16x32_bf16 v[92:95], a[212:215], a[180:183], v[92:95]\n"
    "v_rcp_f32_e32 v8, v32\n"
    "v_mfma_f32_16x16x32_bf16 v[92:95], a[216:219], a[184:187], v[92:95]\n"
    "v_rcp_f32_e32 v9, v33\n"
    "v_mfma_f32_16x16x32_bf16 v[92:95], a[220:223], a[188:191], v[92:95]\n"
    "v_rcp_f32_e32 v10, v34\n"
    "v_mfma_f32_16x16x32_bf16 a[96:99], v[120:123], v[128:131], a[96:99]\n"
    "v_rcp_f32_e32 v11, v35\n"
    "v_mfma_f32_16x16x32_bf16 a[100:103], v[120:123], v[132:135], a[100:103]\n"
    "v_accvgpr_read_b32 v240, a32\n"
    "v_accvgpr_read_b32 v241, a36\n"
    "v_accvgpr_read_b32 v242, a40\n"
    "v_accvgpr_read_b32 v243, a44\n"
    "v_mfma_f32_16x16x32_bf16 a[104:107], v[120:123], v[136:139], a[104:107]\n"
    "v_accvgpr_read_b32 v244, a48\n"
    "v_accvgpr_read_b32 v245, a52\n"
    "v_accvgpr_read_b32 v246, a56\n"
    "v_accvgpr_read_b32 v247, a60\n"
    "v_mfma_f32_16x16x32_bf16 a[108:111], v[120:123], v[140:143], a[108:111]\n"
    "v_mul_f32_e32 v240, v240, v8\n"
    "v_mul_f32_e32 v241, v241, v8\n"
    "v_mul_f32_e32 v242, v242, v8\n"
    "v_mul_f32_e32 v243, v243, v8\n"
    "v_mfma_f32_16x16x32_bf16 a[112:115], v[120:123], v[144:147], a[112:115]\n"
    "v_exp_f32_e32 v88, v88\n"
    "v_mul_f32_e32 v244, v244, v8\n"
    "v_mul_f32_e32 v245, v245, v8\n"
    "v_mfma_f32_16x16x32_bf16 a[116:119], v[120:123], v[148:151], a[116:119]\n"
    "v_mul_f32_e32 v246, v246, v8\n"
    "v_mul_f32_e32 v247, v247, v8\n"
    "v_cvt_pk_bf16_f32 v192, v240, v241\n"
    "v_cvt_pk_bf16_f32 v193, v242, v243\n"
    "v_mfma_f32_16x16x32_bf16 a[120:123], v[120:123], v[152:155], a[120:123]\n"
    "v_exp_f32_e32 v89, v89\n"
    "v_cvt_pk_bf16_f32 v194, v244, v245\n"
    "v_cvt_pk_bf16_f32 v195, v246, v247\n"
    "v_mfma_f32_16x16x32_bf16 a[124:127], v[120:123], v[156:159], a[124:127]\n"
    "global_store_dwordx4 v5, v[192:195], s[62:63] offset:0 sc1\n"
    "v_cvt_pk_bf16_f32 v88, v88, v89\n"
    "v_mfma_f32_16x16x32_bf16 v[40:43], v[120:123], v[24:27], v[40:43]\n"
    "v_exp_f32_e32 v90, v90\n"
    "v_accvgpr_read_b32 v248, a33\n"
    "v_accvgpr_read_b32 v249, a37\n"
    "v_mfma_f32_16x16x32_bf16 v[120:123], v[160:163], a[176:179], v[56:59]\n"
    "v_accvgpr_read_b32 v250, a41\n"
    "v_accvgpr_read_b32 v251, a45\n"
    "v_accvgpr_read_b32 v252, a49\n"
    "v_accvgpr_read_b32 v253, a53\n"
    "v_mfma_f32_16x16x32_bf16 v[120:123], v[164:167], a[180:183], v[120:123]\n"
    "v_exp_f32_e32 v91, v91\n"
    "v_accvgpr_read_b32 v254, a57\n"
    "v_accvgpr_read_b32 v255, a61\n"
    "v_mfma_f32_16x16x32_bf16 v[120:123], v[168:171], a[184:187], v[120:123]\n"
    "v_cvt_pk_bf16_f32 v89, v90, v91\n"
    "v_mul_f32_e32 v248, v248, v9\n"
    "v_mul_f32_e32 v249, v249, v9\n"
    "v_mul_f32_e32 v250, v250, v9\n"
    "v_mfma_f32_16x16x32_bf16 v[120:123], v[172:175], a[188:191], v[120:123]\n"
    "v_exp_f32_e32 v92, v92\n"
    "v_mul_f32_e32 v251, v251, v9\n"
    "v_mul_f32_e32 v252, v252, v9\n"
    "v_mfma_f32_16x16x32_bf16 v[124:127], v[176:179], a[176:179], v[56:59]\n"
    "v_mul_f32_e32 v253, v253, v9\n"
    "v_mul_f32_e32 v254, v254, v9\n"
    "v_mul_f32_e32 v255, v255, v9\n"
    "v_cvt_pk_bf16_f32 v196, v248, v249\n"
    "v_mfma_f32_16x16x32_bf16 v[124:127], v[180:183], a[180:183], v[124:127]\n"
    "v_exp_f32_e32 v93, v93\n"
    "v_cvt_pk_bf16_f32 v197, v250, v251\n"
    "v_cvt_pk_bf16_f32 v198, v252, v253\n"
    "v_mfma_f32_16x16x32_bf16 v[124:127], v[184:187], a[184:187], v[124:127]\n"
    "v_cvt_pk_bf16_f32 v90, v92, v93\n"
    "v_cvt_pk_bf16_f32 v199, v254, v255\n"
    "v_mfma_f32_16x16x32_bf16 v[124:127], v[188:191], a[188:191], v[124:127]\n"
    "global_store_dwordx4 v5, v[196:199], s[62:63] offset:256 sc1\n"
    "v_exp_f32_e32 v94, v94\n"
    "v_mfma_f32_16x16x32_bf16 a[64:67], v[84:87], v[208:211], a[64:67]\n"
    "v_accvgpr_read_b32 v240, a34\n"
    "v_accvgpr_read_b32 v241, a38\n"
    "v_accvgpr_read_b32 v242, a42\n"
    "v_accvgpr_read_b32 v243, a46\n"
    "v_mfma_f32_16x16x32_bf16 a[68:71], v[84:87], v[212:215], a[68:71]\n"
    "v_exp_f32_e32 v95, v95\n"
    "v_accvgpr_read_b32 v244, a50\n"
    "v_accvgpr_read_b32 v245, a54\n"
    "v_mfma_f32_16x16x32_bf16 a[72:75], v[84:87], v[216:219], a[72:75]\n"
    "v_cvt_pk_bf16_f32 v91, v94, v95\n"
    "v_accvgpr_read_b32 v246, a58\n"
    "v_accvgpr_read_b32 v247, a62\n"
    "v_mul_f32_e32 v240, v240, v10\n"
    "v_mfma_f32_16x16x32_bf16 a[76:79], v[84:87], v[220:223], a[76:79]\n"
    "v_mul_f32_e32 v241, v241, v10\n"
    "v_mul_f32_e32 v242, v242, v10\n"
    "v_mul_f32_e32 v243, v243, v10\n"
    "v_mul_f32_e32 v244, v244, v10\n"
    "v_mfma_f32_16x16x32_bf16 a[80:83], v[84:87], v[224:227], a[80:83]\n"
    "v_exp_f32_e32 v120, v120\n"
    "v_mul_f32_e32 v245, v245, v10\n"
    "v_mul_f32_e32 v246, v246, v10\n"
    "v_mfma_f32_16x16x32_bf16 a[84:87], v[84:87], v[228:231], a[84:87]\n"
    "v_mul_f32_e32 v247, v247, v10\n"
    "v_cvt_pk_bf16_f32 v192, v240, v241\n"
    "v_cvt_pk_bf16_f32 v193, v242, v243\n"
    "v_cvt_pk_bf16_f32 v194, v244, v245\n"
    "v_mfma_f32_16x16x32_bf16 a[88:91], v[84:87], v[232:235], a[88:91]\n"
    "v_exp_f32_e32 v121, v121\n"
    "v_cvt_pk_bf16_f32 v195, v246, v247\n"
    "v_mfma_f32_16x16x32_bf16 a[92:95], v[84:87], v[236:239], a[92:95]\n"
    "global_store_dwordx4 v5, v[192:195], s[62:63] offset:512 sc1\n"
    "v_cvt_pk_bf16_f32 v120, v120, v121\n"
    "v_mfma_f32_16x16x32_bf16 v[36:39], v[84:87], v[24:27], v[36:39]\n"
    "v_exp_f32_e32 v122, v122\n"
    "v_accvgpr_read_b32 v248, a35\n"
    "v_accvgpr_read_b32 v249, a39\n"
    "v_mfma_f32_16x16x32_bf16 a[64:67], v[116:119], a[224:227], a[64:67]\n"
    "v_accvgpr_read_b32 v250, a43\n"
    "v_accvgpr_read_b32 v251, a47\n"
    "v_accvgpr_read_b32 v252, a51\n"
    "v_accvgpr_read_b32 v253, a55\n"
    "v_mfma_f32_16x16x32_bf16 a[68:71], v[116:119], a[228:231], a[68:71]\n"
    "v_exp_f32_e32 v123, v123\n"
    "v_accvgpr_read_b32 v254, a59\n"
    "v_accvgpr_read_b32 v255, a63\n"
    "v_mfma_f32_16x16x32_bf16 a[72:75], v[116:119], a[232:235], a[72:75]\n"
    "v_cvt_pk_bf16_f32 v121, v122, v123\n"
    "v_mul_f32_e32 v248, v248, v11\n"
    "v_mul_f32_e32 v249, v249, v11\n"
    "v_mul_f32_e32 v250, v250, v11\n"
    "v_mfma_f32_16x16x32_bf16 a[76:79], v[116:119], a[236:239], a[76:79]\n"
    "v_exp_f32_e32 v124, v124\n"
    "v_mul_f32_e32 v251, v251, v11\n"
    "v_mul_f32_e32 v252, v252, v11\n"
    "v_mfma_f32_16x16x32_bf16 a[80:83], v[116:119], a[240:243], a[80:83]\n"
    "v_mul_f32_e32 v253, v253, v11\n"
    "v_mul_f32_e32 v254, v254, v11\n"
    "v_mul_f32_e32 v255, v255, v11\n"
    "v_cvt_pk_bf16_f32 v196, v248, v249\n"
    "v_mfma_f32_16x16x32_bf16 a[84:87], v[116:119], a[244:247], a[84:87]\n"
    "v_exp_f32_e32 v125, v125\n"
    "v_cvt_pk_bf16_f32 v197, v250, v251\n"
    "v_cvt_pk_bf16_f32 v198, v252, v253\n"
    "v_mfma_f32_16x16x32_bf16 a[88:91], v[116:119], a[248:251], a[88:91]\n"
    "v_cvt_pk_bf16_f32 v122, v124, v125\n"
    "v_cvt_pk_bf16_f32 v199, v254, v255\n"
    "v_mfma_f32_16x16x32_bf16 a[92:95], v[116:119], a[252:255], a[92:95]\n"
    "global_store_dwordx4 v5, v[196:199], s[62:63] offset:768 sc1\n"
    "v_exp_f32_e32 v126, v126\n"
    "v_mfma_f32_16x16x32_bf16 v[36:39], v[116:119], v[24:27], v[36:39]\n"
    "v_mfma_f32_16x16x32_bf16 a[96:99], v[88:91], v[208:211], a[96:99]\n"
    "v_exp_f32_e32 v127, v127\n"
    "v_mfma_f32_16x16x32_bf16 a[100:103], v[88:91], v[212:215], a[100:103]\n"
    "v_cvt_pk_bf16_f32 v123, v126, v127\n"
    "v_mfma_f32_16x16x32_bf16 a[104:107], v[88:91], v[216:219], a[104:107]\n"
    "v_mfma_f32_16x16x32_bf16 a[108:111], v[88:91], v[220:223], a[108:111]\n"
    "v_mfma_f32_16x16x32_bf16 a[112:115], v[88:91], v[224:227], a[112:115]\n"
    "v_mfma_f32_16x16x32_bf16 a[116:119], v[88:91], v[228:231], a[116:119]\n"
    "v_rcp_f32_e32 v8, v36\n"
    "v_mfma_f32_16x16x32_bf16 a[120:123], v[88:91], v[232:235], a[120:123]\n"
    "v_rcp_f32_e32 v9, v37\n"
    "v_mfma_f32_16x16x32_bf16 a[124:127], v[88:91], v[236:239], a[124:127]\n"
    "v_rcp_f32_e32 v10, v38\n"
    "v_mfma_f32_16x16x32_bf16 v[40:43], v[88:91], v[24:27], v[40:43]\n"
    "v_rcp_f32_e32 v11, v39\n"
    "v_mfma_f32_16x16x32_bf16 a[96:99], v[120:123], a[224:227], a[96:99]\n"
    "v_accvgpr_read_b32 v240, a64\n"
    "v_accvgpr_read_b32 v241, a68\n"
    "v_accvgpr_read_b32 v242, a72\n"
    "v_accvgpr_read_b32 v243, a76\n"
    "v_mfma_f32_16x16x32_bf16 a[100:103], v[120:123], a[228:231], a[100:103]\n"
    "v_accvgpr_read_b32 v244, a80\n"
    "v_accvgpr_read_b32 v245, a84\n"
    "v_accvgpr_read_b32 v246, a88\n"
    "v_accvgpr_read_b32 v247, a92\n"
    "v_mfma_f32_16x16x32_bf16 a[104:107], v[120:123], a[232:235], a[104:107]\n"
    "v_mul_f32_e32 v240, v240, v8\n"
    "v_mul_f32_e32 v241, v241, v8\n"
    "v_mul_f32_e32 v242, v242, v8\n"
    "v_mul_f32_e32 v243, v243, v8\n"
    "v_mfma_f32_16x16x32_bf16 a[108:111], v[120:123], a[236:239], a[108:111]\n"
    "v_mul_f32_e32 v244, v244, v8\n"
    "v_mul_f32_e32 v245, v245, v8\n"
    "v_mul_f32_e32 v246, v246, v8\n"
    "v_mul_f32_e32 v247, v247, v8\n"
    "v_mfma_f32_16x16x32_bf16 a[112:115], v[120:123], a[240:243], a[112:115]\n"
    "v_cvt_pk_bf16_f32 v192, v240, v241\n"
    "v_cvt_pk_bf16_f32 v193, v242, v243\n"
    "v_cvt_pk_bf16_f32 v194, v244, v245\n"
    "v_cvt_pk_bf16_f32 v195, v246, v247\n"
    "v_mfma_f32_16x16x32_bf16 a[116:119], v[120:123], a[244:247], a[116:119]\n"
    "global_store_dwordx4 v6, v[192:195], s[62:63] offset:-4096 sc1\n"
    "v_mfma_f32_16x16x32_bf16 a[120:123], v[120:123], a[248:251], a[120:123]\n"
    "v_accvgpr_read_b32 v248, a65\n"
    "v_accvgpr_read_b32 v249, a69\n"
    "v_accvgpr_read_b32 v250, a73\n"
    "v_accvgpr_read_b32 v251, a77\n"
    "v_mfma_f32_16x16x32_bf16 a[124:127], v[120:123], a[252:255], a[124:127]\n"
    "v_accvgpr_read_b32 v252, a81\n"
    "v_accvgpr_read_b32 v253, a85\n"
    "v_accvgpr_read_b32 v254, a89\n"
    "v_accvgpr_read_b32 v255, a93\n"
    "v_mfma_f32_16x16x32_bf16 v[40:43], v[120:123], v[24:27], v[40:43]\n"
    "v_mul_f32_e32 v248, v248, v9\n"
    "v_mul_f32_e32 v249, v249, v9\n"
    "v_mul_f32_e32 v250, v250, v9\n"
    "v_mul_f32_e32 v251, v251, v9\n"
    "s_mov_b32 s66, 0x69800000\n"
    "v_mul_f32_e32 v252, v252, v9\n"
    "v_mul_f32_e32 v253, v253, v9\n"
    "v_mul_f32_e32 v254, v254, v9\n"
    "v_mul_f32_e32 v255, v255, v9\n"
    "v_cvt_pk_bf16_f32 v196, v248, v249\n"
    "v_cvt_pk_bf16_f32 v197, v250, v251\n"
    "v_cvt_pk_bf16_f32 v198, v252, v253\n"
    "v_cvt_pk_bf16_f32 v199, v254, v255\n"
    "global_store_dwordx4 v6, v[196:199], s[62:63] offset:-3840 sc1\n"
    "v_accvgpr_read_b32 v240, a66\n"
    "v_accvgpr_read_b32 v241, a70\n"
    "v_accvgpr_read_b32 v242, a74\n"
    "v_accvgpr_read_b32 v243, a78\n"
    "v_accvgpr_read_b32 v244, a82\n"
    "v_accvgpr_read_b32 v245, a86\n"
    "v_accvgpr_read_b32 v246, a90\n"
    "v_accvgpr_read_b32 v247, a94\n"
    "v_mul_f32_e32 v240, v240, v10\n"
    "v_mul_f32_e32 v241, v241, v10\n"
    "v_mul_f32_e32 v242, v242, v10\n"
    "v_mul_f32_e32 v243, v243, v10\n"
    "v_mul_f32_e32 v244, v244, v10\n"
    "v_mul_f32_e32 v245, v245, v10\n"
    "v_mul_f32_e32 v246, v246, v10\n"
    "v_mul_f32_e32 v247, v247, v10\n"
    "v_cvt_pk_bf16_f32 v192, v240, v241\n"
    "v_cvt_pk_bf16_f32 v193, v242, v243\n"
    "v_cvt_pk_bf16_f32 v194, v244, v245\n"
    "v_cvt_pk_bf16_f32 v195, v246, v247\n"
    "global_store_dwordx4 v6, v[192:195], s[62:63] offset:-3584 sc1\n"
    "v_accvgpr_read_b32 v248, a67\n"
    "v_accvgpr_read_b32 v249, a71\n"
    "v_accvgpr_read_b32 v250, a75\n"
    "v_accvgpr_read_b32 v251, a79\n"
    "v_accvgpr_read_b32 v252, a83\n"
    "v_accvgpr_read_b32 v253, a87\n"
    "v_accvgpr_read_b32 v254, a91\n"
    "v_accvgpr_read_b32 v255, a95\n"
    "v_mul_f32_e32 v248, v248, v11\n"
    "v_mul_f32_e32 v249, v249, v11\n"
    "v_mul_f32_e32 v250, v250, v11\n"
    "v_mul_f32_e32 v251, v251, v11\n"
    "v_mul_f32_e32 v252, v252, v11\n"
    "v_mul_f32_e32 v253, v253, v11\n"
    "v_mul_f32_e32 v254, v254, v11\n"
    "v_mul_f32_e32 v255, v255, v11\n"
    "v_cvt_pk_bf16_f32 v196, v248, v249\n"
    "v_cvt_pk_bf16_f32 v197, v250, v251\n"
    "v_cvt_pk_bf16_f32 v198, v252, v253\n"
    "v_cvt_pk_bf16_f32 v199, v254, v255\n"
    "global_store_dwordx4 v6, v[196:199], s[62:63] offset:-3328 sc1\n"
    "s_nop 7\n"
    "v_rcp_f32_e32 v8, v40\n"
    "v_rcp_f32_e32 v9, v41\n"
    "v_rcp_f32_e32 v10, v42\n"
    "v_rcp_f32_e32 v11, v43\n"
    "v_accvgpr_read_b32 v240, a96\n"
    "v_accvgpr_read_b32 v241, a100\n"
    "v_accvgpr_read_b32 v242, a104\n"
    "v_accvgpr_read_b32 v243, a108\n"
    "v_accvgpr_read_b32 v244, a112\n"
    "v_accvgpr_read_b32 v245, a116\n"
    "v_accvgpr_read_b32 v246, a120\n"
    "v_accvgpr_read_b32 v247, a124\n"
    "v_pk_mul_f32 v[240:241], v[240:241], v[8:9] op_sel:[0,0] op_sel_hi:[1,0]\n"
    "v_pk_mul_f32 v[242:243], v[242:243], v[8:9] op_sel:[0,0] op_sel_hi:[1,0]\n"
    "v_pk_mul_f32 v[244:245], v[244:245], v[8:9] op_sel:[0,0] op_sel_hi:[1,0]\n"
    "v_pk_mul_f32 v[246:247], v[246:247], v[8:9] op_sel:[0,0] op_sel_hi:[1,0]\n"
    "v_cvt_pk_bf16_f32 v192, v240, v241\n"
    "v_cvt_pk_bf16_f32 v193, v242, v243\n"
    "v_cvt_pk_bf16_f32 v194, v244, v245\n"
    "v_cvt_pk_bf16_f32 v195, v246, v247\n"
    "global_store_dwordx4 v6, v[192:195], s[62:63] offset:0 sc1\n"
    "v_accvgpr_read_b32 v248, a97\n"
    "v_accvgpr_read_b32 v249, a101\n"
    "v_accvgpr_read_b32 v250, a105\n"
    "v_accvgpr_read_b32 v251, a109\n"
    "v_accvgpr_read_b32 v252, a113\n"
    "v_accvgpr_read_b32 v253, a117\n"
    "v_accvgpr_read_b32 v254, a121\n"
    "v_accvgpr_read_b32 v255, a125\n"
    "v_pk_mul_f32 v[248:249], v[248:249], v[8:9] op_sel:[0,1] op_sel_hi:[1,1]\n"
    "v_pk_mul_f32 v[250:251], v[250:251], v[8:9] op_sel:[0,1] op_sel_hi:[1,1]\n"
    "v_pk_mul_f32 v[252:253], v[252:253], v[8:9] op_sel:[0,1] op_sel_hi:[1,1]\n"
    "v_pk_mul_f32 v[254:255], v[254:255], v[8:9] op_sel:[0,1] op_sel_hi:[1,1]\n"
    "v_cvt_pk_bf16_f32 v196, v248, v249\n"
    "v_cvt_pk_bf16_f32 v197, v250, v251\n"
    "v_cvt_pk_bf16_f32 v198, v252, v253\n"
    "v_cvt_pk_bf16_f32 v199, v254, v255\n"
    "global_store_dwordx4 v6, v[196:199], s[62:63] offset:256 sc1\n"
    "v_accvgpr_read_b32 v240, a98\n"
    "v_accvgpr_read_b32 v241, a102\n"
    "v_accvgpr_read_b32 v242, a106\n"
    "v_accvgpr_read_b32 v243, a110\n"
    "v_accvgpr_read_b32 v244, a114\n"
    "v_accvgpr_read_b32 v245, a118\n"
    "v_accvgpr_read_b32 v246, a122\n"
    "v_accvgpr_read_b32 v247, a126\n"
    "v_pk_mul_f32 v[240:241], v[240:241], v[10:11] op_sel:[0,0] op_sel_hi:[1,0]\n"
    "v_pk_mul_f32 v[242:243], v[242:243], v[10:11] op_sel:[0,0] op_sel_hi:[1,0]\n"
    "v_pk_mul_f32 v[244:245], v[244:245], v[10:11] op_sel:[0,0] op_sel_hi:[1,0]\n"
    "v_pk_mul_f32 v[246:247], v[246:247], v[10:11] op_sel:[0,0] op_sel_hi:[1,0]\n"
    "v_cvt_pk_bf16_f32 v192, v240, v241\n"
    "v_cvt_pk_bf16_f32 v193, v242, v243\n"
    "v_cvt_pk_bf16_f32 v194, v244, v245\n"
    "v_cvt_pk_bf16_f32 v195, v246, v247\n"
    "global_store_dwordx4 v6, v[192:195], s[62:63] offset:512 sc1\n"
    "v_accvgpr_read_b32 v248, a99\n"
    "v_accvgpr_read_b32 v249, a103\n"
    "v_accvgpr_read_b32 v250, a107\n"
    "v_accvgpr_read_b32 v251, a111\n"
    "v_accvgpr_read_b32 v252, a115\n"
    "v_accvgpr_read_b32 v253, a119\n"
    "v_accvgpr_read_b32 v254, a123\n"
    "v_accvgpr_read_b32 v255, a127\n"
    "v_pk_mul_f32 v[248:249], v[248:249], v[10:11] op_sel:[0,1] op_sel_hi:[1,1]\n"
    "v_pk_mul_f32 v[250:251], v[250:251], v[10:11] op_sel:[0,1] op_sel_hi:[1,1]\n"
    "v_pk_mul_f32 v[252:253], v[252:253], v[10:11] op_sel:[0,1] op_sel_hi:[1,1]\n"
    "v_pk_mul_f32 v[254:255], v[254:255], v[10:11] op_sel:[0,1] op_sel_hi:[1,1]\n"
    "v_cvt_pk_bf16_f32 v196, v248, v249\n"
    "v_cvt_pk_bf16_f32 v197, v250, v251\n"
    "v_cvt_pk_bf16_f32 v198, v252, v253\n"
    "v_cvt_pk_bf16_f32 v199, v254, v255\n"
    "global_store_dwordx4 v6, v[196:199], s[62:63] offset:768 sc1\n"
    "v_add_f32_e32 v13, v28, v29\n"
    "v_add_f32_e32 v13, v13, v30\n"
    "v_add_f32_e32 v13, v13, v31\n"
    "v_add_f32_e32 v13, v13, v32\n"
    "v_add_f32_e32 v13, v13, v33\n"
    "v_add_f32_e32 v13, v13, v34\n"
    "v_add_f32_e32 v13, v13, v35\n"
    "v_add_f32_e32 v13, v13, v36\n"
    "v_add_f32_e32 v13, v13, v37\n"
    "v_add_f32_e32 v13, v13, v38\n"
    "v_add_f32_e32 v13, v13, v39\n"
    "v_add_f32_e32 v13, v13, v40\n"
    "v_add_f32_e32 v13, v13, v41\n"
    "v_add_f32_e32 v13, v13, v42\n"
    "v_add_f32_e32 v13, v13, v43\n"
    "v_cmp_nge_f32_e32 vcc, s66, v13\n"
    "s_mov_b64 s[44:45], vcc\n"
    "s_waitcnt lgkmcnt(0)\n"
    "v_cmp_ne_u32_e32 vcc, 0, v12\n"
    "s_or_b64 s[44:45], s[44:45], vcc\n"
    "s_or_b32 s99, s44, s45\n"
    "s_waitcnt vmcnt(0)\n"
    "s_mov_b32 %[bad], s99\n"
    "s_branch .Lattn_end\n"
    ".Lspin_l0:\n"
    "v_cmp_lt_u32_e32 vcc, s96, v23\n"
    "s_cbranch_vccz .Lsrd_l0\n"
    "s_sub_u32 s97, s97, 1\n"
    "s_cbranch_scc0 .Lsgo_l0\n"
    "v_mov_b32_e32 v61, 0x7fffff00\n"
    "v_mov_b32_e32 v23, 0x25000\n"
    "ds_write_b32 v23, v23\n"
    ".Lsgo_l0:\n"
    "buffer_load_dword v23, v61, s[88:91], 0 offen sc1\n"
    "s_waitcnt vmcnt(0)\n"
    "v_max_u32_e32 v23, s96, v23\n"
    "ds_write_b32 v229, v23\n"
    ".Lsrd_l0:\n"
    "ds_read_b32 v231, v230\n"
    "s_waitcnt lgkmcnt(0)\n"
    "v_cmp_lt_u32_e32 vcc, s96, v231\n"
    "s_cbranch_vccnz .Lspin_l0\n"
    "s_branch .Lback_l0\n"
    ".Lspin_l1:\n"
    "v_cmp_lt_u32_e32 vcc, s96, v23\n"
    "s_cbranch_vccz .Lsrd_l1\n"
    "s_sub_u32 s97, s97, 1\n"
    "s_cbranch_scc0 .Lsgo_l1\n"
    "v_mov_b32_e32 v61, 0x7fffff00\n"
    "v_mov_b32_e32 v23, 0x25000\n"
    "ds_write_b32 v23, v23\n"
    ".Lsgo_l1:\n"
    "buffer_load_dword v23, v61, s[88:91], 0 offen sc1\n"
    "s_waitcnt vmcnt(0)\n"
    "v_max_u32_e32 v23, s96, v23\n"
    "ds_write_b32 v229, v23\n"
    ".Lsrd_l1:\n"
    "ds_read_b32 v231, v230\n"
    "s_waitcnt lgkmcnt(0)\n"
    "v_cmp_lt_u32_e32 vcc, s96, v231\n"
    "s_cbranch_vccnz .Lspin_l1\n"
    "s_branch .Lback_l1\n"
    ".Lspin_l2:\n"
    "v_cmp_lt_u32_e32 vcc, s96, v23\n"
    "s_cbranch_vccz .Lsrd_l2\n"
    "s_sub_u32 s97, s97, 1\n"
    "s_cbranch_scc0 .Lsgo_l2\n"
    "v_mov_b32_e32 v61, 0x7fffff00\n"
    "v_mov_b32_e32 v23, 0x25000\n"
    "ds_write_b32 v23, v23\n"
    ".Lsgo_l2:\n"
    "buffer_load_dword v23, v61, s[88:91], 0 offen sc1\n"
    "s_waitcnt vmcnt(0)\n"
    "v_max_u32_e32 v23, s96, v23\n"
    "ds_write_b32 v229, v23\n"
    ".Lsrd_l2:\n"
    "ds_read_b32 v231, v230\n"
    "s_waitcnt lgkmcnt(0)\n"
    "v_cmp_lt_u32_e32 vcc, s96, v231\n"
    "s_cbranch_vccnz .Lspin_l2\n"
    "s_branch .Lback_l2\n"
    ".Lspin_l3:\n"
    "v_cmp_lt_u32_e32 vcc, s96, v23\n"
    "s_cbranch_vccz .Lsrd_l3\n"
    "s_sub_u32 s97, s97, 1\n"
    "s_cbranch_scc0 .Lsgo_l3\n"
    "v_mov_b32_e32 v61, 0x7fffff00\n"
    "v_mov_b32_e32 v23, 0x25000\n"
    "ds_write_b32 v23, v23\n"
    ".Lsgo_l3:\n"
    "buffer_load_dword v23, v61, s[88:91], 0 offen sc1\n"
    "s_waitcnt vmcnt(0)\n"
    "v_max_u32_e32 v23, s96, v23\n"
    "ds_write_b32 v229, v23\n"
    ".Lsrd_l3:\n"
    "ds_read_b32 v231, v230\n"
    "s_waitcnt lgkmcnt(0)\n"
    "v_cmp_lt_u32_e32 vcc, s96, v231\n"
    "s_cbranch_vccnz .Lspin_l3\n"
    "s_branch .Lback_l3\n"
    ".Lspin_p24:\n"
    "v_cmp_lt_u32_e32 vcc, s96, v23\n"
    "s_cbranch_vccz .Lsrd_p24\n"
    "s_sub_u32 s97, s97, 1\n"
    "s_cbranch_scc0 .Lsgo_p24\n"
    "v_mov_b32_e32 v61, 0x7fffff00\n"
    "v_mov_b32_e32 v23, 0x25000\n"
    "ds_write_b32 v23, v23\n"
    ".Lsgo_p24:\n"
    "buffer_load_dword v23, v61, s[88:91], 0 offen sc1\n"
    "s_waitcnt vmcnt(0)\n"
    "v_max_u32_e32 v23, s96, v23\n"
    "ds_write_b32 v229, v23\n"
    ".Lsrd_p24:\n"
    "ds_read_b32 v231, v230\n"
    "s_waitcnt lgkmcnt(0)\n"
    "v_cmp_lt_u32_e32 vcc, s96, v231\n"
    "s_cbranch_vccnz .Lspin_p24\n"
    "s_branch .Lback_p24\n"
    ".Lspin_p25:\n"
    "v_cmp_lt_u32_e32 vcc, s96, v23\n"
    "s_cbranch_vccz .Lsrd_p25\n"
    "s_sub_u32 s97, s97, 1\n"
    "s_cbranch_scc0 .Lsgo_p25\n"
    "v_mov_b32_e32 v61, 0x7fffff00\n"
    "v_mov_b32_e32 v23, 0x25000\n"
    "ds_write_b32 v23, v23\n"
    ".Lsgo_p25:\n"
    "buffer_load_dword v23, v61, s[88:91], 0 offen sc1\n"
    "s_waitcnt vmcnt(0)\n"
    "v_max_u32_e32 v23, s96, v23\n"
    "ds_write_b32 v229, v23\n"
    ".Lsrd_p25:\n"
    "ds_read_b32 v231, v230\n"
    "s_waitcnt lgkmcnt(0)\n"
    "v_cmp_lt_u32_e32 vcc, s96, v231\n"
    "s_cbranch_vccnz .Lspin_p25\n"
    "s_branch .Lback_p25\n"
    ".Lspin_p26:\n"
    "v_cmp_lt_u32_e32 vcc, s96, v23\n"
    "s_cbranch_vccz .Lsrd_p26\n"
    "s_sub_u32 s97, s97, 1\n"
    "s_cbranch_scc0 .Lsgo_p26\n"
    "v_mov_b32_e32 v61, 0x7fffff00\n"
    "v_mov_b32_e32 v23, 0x25000\n"
    "ds_write_b32 v23, v23\n"
    ".Lsgo_p26:\n"
    "buffer_load_dword v23, v61, s[88:91], 0 offen sc1\n"
    "s_waitcnt vmcnt(0)\n"
    "v_max_u32_e32 v23, s96, v23\n"
    "ds_write_b32 v229, v23\n"
    ".Lsrd_p26:\n"
    "ds_read_b32 v231, v230\n"
    "s_waitcnt lgkmcnt(0)\n"
    "v_cmp_lt_u32_e32 vcc, s96, v231\n"
    "s_cbranch_vccnz .Lspin_p26\n"
    "s_branch .Lback_p26\n"
    ".Lspin_p27:\n"
    "v_cmp_lt_u32_e32 vcc, s96, v23\n"
    "s_cbranch_vccz .Lsrd_p27\n"
    "s_sub_u32 s97, s97, 1\n"
    "s_cbranch_scc0 .Lsgo_p27\n"
    "v_mov_b32_e32 v61, 0x7fffff00\n"
    "v_mov_b32_e32 v23, 0x25000\n"
    "ds_write_b32 v23, v23\n"
    ".Lsgo_p27:\n"
    "buffer_load_dword v23, v61, s[88:91], 0 offen sc1\n"
    "s_waitcnt vmcnt(0)\n"
    "v_max_u32_e32 v23, s96, v23\n"
    "ds_write_b32 v229, v23\n"
    ".Lsrd_p27:\n"
    "ds_read_b32 v231, v230\n"
    "s_waitcnt lgkmcnt(0)\n"
    "v_cmp_lt_u32_e32 vcc, s96, v231\n"
    "s_cbranch_vccnz .Lspin_p27\n"
    "s_branch .Lback_p27\n"
    ".Lspin_p28:\n"
    "v_cmp_lt_u32_e32 vcc, s96, v23\n"
    "s_cbranch_vccz .Lsrd_p28\n"
    "s_sub_u32 s97, s97, 1\n"
    "s_cbranch_scc0 .Lsgo_p28\n"
    "v_mov_b32_e32 v61, 0x7fffff00\n"
    "v_mov_b32_e32 v23, 0x25000\n"
    "ds_write_b32 v23, v23\n"
    ".Lsgo_p28:\n"
    "buffer_load_dword v23, v61, s[88:91], 0 offen sc1\n"
    "s_waitcnt vmcnt(0)\n"
    "v_max_u32_e32 v23, s96, v23\n"
    "ds_write_b32 v229, v23\n"
    ".Lsrd_p28:\n"
    "ds_read_b32 v231, v230\n"
    "s_waitcnt lgkmcnt(0)\n"
    "v_cmp_lt_u32_e32 vcc, s96, v231\n"
    "s_cbranch_vccnz .Lspin_p28\n"
    "s_branch .Lback_p28\n"
    ".Lattn_end:\n"
    : [bad] "=s"(bad) : [karg] "s"(karg), [wg] "s"(wg), [wave] "s"(wave), [h] "s"(h), [j] "s"(j) : "v2", "v3", "v4", "v5", "v6", "v7", "v8", "v9", "v10", "v11", "v12", "v13", "v14", "v15", "v16", "v17", "v18", "v19", "v20", "v21", "v22", "v23", "v24", "v25", "v26", "v27", "v28", "v29", "v30", "v31", "v32", "v33", "v34", "v35", "v36", "v37", "v38", "v39", "v40", "v41", "v42", "v43", "v44", "v45", "v46", "v47", "v48", "v49", "v50", "v51", "v52", "v53", "v54", "v55", "v56", "v57", "v58", "v59", "v60", "v61", "v62", "v63", "v64", "v65", "v66", "v67", "v68", "v69", "v70", "v71", "v72", "v73", "v74", "v75", "v76", "v77", "v78", "v79", "v80", "v81", "v82", "v83", "v84", "v85", "v86", "v87", "v88", "v89", "v90", "v91", "v92", "v93", "v94", "v95", "v96", "v97", "v98", "v99", "v100", "v101", "v102", "v103", "v104", "v105", "v106", "v107", "v108", "v109", "v110", "v111", "v112", "v113", "v114", "v115", "v116", "v117", "v118", "v119", "v120", "v121", "v122", "v123", "v124", "v125", "v126", "v127", "v128", "v129", "v130", "v131", "v132", "v133", "v134", "v135", "v136", "v137", "v138", "v139", "v140", "v141", "v142", "v143", "v144", "v145", "v146", "v147", "v148", "v149", "v150", "v151", "v152", "v153", "v154", "v155", "v156", "v157", "v158", "v159", "v160", "v161", "v162", "v163", "v164", "v165", "v166", "v167", "v168", "v169", "v170", "v171", "v172", "v173", "v174", "v175", "v176", "v177", "v178", "v179", "v180", "v181", "v182", "v183", "v184", "v185", "v186", "v187", "v188", "v189", "v190", "v191", "v192", "v193", "v194", "v195", "v196", "v197", "v198", "v199", "v200", "v201", "v202", "v203", "v204", "v205", "v206", "v207", "v208", "v209", "v210", "v211", "v212", "v213", "v214", "v215", "v216", "v217", "v218", "v219", "v220", "v221", "v222", "v223", "v224", "v225", "v226", "v227", "v228", "v229", "v230", "v231", "v232", "v233", "v234", "v235", "v236", "v237", "v238", "v239", "v240", "v241", "v242", "v243", "v244", "v245", "v246", "v247", "v248", "v249", "v250", "v251", "v252", "v253", "v254", "v255", "a0", "a1", "a2", "a3", "a4", "a5", "a6", "a7", "a8", "a9", "a10", "a11", "a12", "a13", "a14", "a15", "a16", "a17", "a18", "a19", "a20", "a21", "a22", "a23", "a24", "a25", "a26", "a27", "a28", "a29", "a30", "a31", "a32", "a33", "a34", "a35", "a36", "a37", "a38", "a39", "a40", "a41", "a42", "a43", "a44", "a45", "a46", "a47", "a48", "a49", "a50", "a51", "a52", "a53", "a54", "a55", "a56", "a57", "a58", "a59", "a60", "a61", "a62", "a63", "a64", "a65", "a66", "a67", "a68", "a69", "a70", "a71", "a72", "a73", "a74", "a75", "a76", "a77", "a78", "a79", "a80", "a81", "a82", "a83", "a84", "a85", "a86", "a87", "a88", "a89", "a90", "a91", "a92", "a93", "a94", "a95", "a96", "a97", "a98", "a99", "a100", "a101", "a102", "a103", "a104", "a105", "a106", "a107", "a108", "a109", "a110", "a111", "a112", "a113", "a114", "a115", "a116", "a117", "a118", "a119", "a120", "a121", "a122", "a123", "a124", "a125", "a126", "a127", "a128", "a129", "a130", "a131", "a132", "a133", "a134", "a135", "a136", "a137", "a138", "a139", "a140", "a141", "a142", "a143", "a144", "a145", "a146", "a147", "a148", "a149", "a150", "a151", "a152", "a153", "a154", "a155", "a156", "a157", "a158", "a159", "a160", "a161", "a162", "a163", "a164", "a165", "a166", "a167", "a168", "a169", "a170", "a171", "a172", "a173", "a174", "a175", "a176", "a177", "a178", "a179", "a180", "a181", "a182", "a183", "a184", "a185", "a186", "a187", "a188", "a189", "a190", "a191", "a192", "a193", "a194", "a195", "a196", "a197", "a198", "a199", "a200", "a201", "a202", "a203", "a204", "a205", "a206", "a207", "a208", "a209", "a210", "a211", "a212", "a213", "a214", "a215", "a216", "a217", "a218", "a219", "a220", "a221", "a222", "a223", "a224", "a225", "a226", "a227", "a228", "a229", "a230", "a231", "a232", "a233", "a234", "a235", "a236", "a237", "a238", "a239", "a240", "a241", "a242", "a243", "a244", "a245", "a246", "a247", "a248", "a249", "a250", "a251", "a252", "a253", "a254", "a255", "s28", "s29", "s30", "s31", "s34", "s35", "s36", "s37", "s38", "s39", "s40", "s41", "s42", "s43", "s44", "s45", "s46", "s47", "s48", "s49", "s50", "s51", "s52", "s53", "s54", "s55", "s56", "s57", "s58", "s59", "s60", "s61", "s62", "s63", "s64", "s65", "s66", "s67", "s68", "s69", "s70", "s71", "s72", "s73", "s74", "s75", "s76", "s77", "s78", "s79", "s80", "s81", "s82", "s83", "s84", "s85", "s86", "s87", "s88", "s89", "s90", "s91", "s92", "s93", "s94", "s95", "s96", "s97", "s98", "s99", "vcc", "scc", "memory");
  if (bad) {
    typedef const u16* cu;
    slow_rows_body((cu)q, (const int*)qk, (cu)ks, (cu)kz, (const int*)qv, (cu)vs, (cu)vz, (cu)qc, (cu)qs, (cu)kc, (cu)ksn, (u16*)out, h, j * 256u + wave * 64u);
  }
}

extern "C" void kernel_launch(void* const* d_in, const int* in_sizes, int n_in, void* d_out, int out_size, void* d_ws, size_t ws_size, hipStream_t stream) {
  (void)hipFuncSetAttribute((const void*)attn_main, hipFuncAttributeMaxDynamicSharedMemorySize, 163840);
  hipLaunchKernelGGL(attn_main, dim3(256), dim3(256), 163840, stream, d_in[0], d_in[1], d_in[2], d_in[3], d_in[4], d_in[5], d_in[6], d_in[7], d_in[8], d_in[9], d_in[10], d_out, d_ws);
}
```
